```python
import torch

def _softmax(x, axis=-1):
    return torch.softmax(x, dim=axis)
def _gelu(x):
    return torch.nn.functional.gelu(x, approximate="tanh")
def _silu(x):
    return torch.nn.functional.silu(x)
def _sigmoid(x):
    return torch.sigmoid(x)
def _layernorm(x, axis=-1, eps=1e-5):
    m = x.mean(dim=axis, keepdim=True)
    v = x.var(dim=axis, keepdim=True, unbiased=False)
    return (x - m) / torch.sqrt(v + eps)
def _scatter_add(tgt, idx, src, axis=0):
    return tgt.clone().index_add_(axis, idx, src)
def _segment_sum(src, seg, n):
    out = torch.zeros((n,) + src.shape[1:], dtype=src.dtype)
    return out.index_add_(0, seg, src)
def _pad(x, pw):
    flat = tuple(v for pair in reversed(pw) for v in pair)
    return torch.nn.functional.pad(x, flat)

kernel_name = 'concat_heads__gqa_block_cached__gqa_block_cached__near_ridge'

def setup_inputs():

    _w = (lambda a: (lambda t: t.to(torch.bfloat16) if t.is_floating_point() else t)(torch.as_tensor(a)))
    def _A_setup():
        B, Sq, D = 16, 48, 512
        Hq, Hkv, Sk, Dh = 32, 8, 512, 128
        assert Hq % Hkv == 0, (Hq, Hkv)
        return {
            "x": _w((lambda *s: torch.randn(*s, dtype=torch.bfloat16) * 0.8)(B, Sq, D)),
            "k_cache": _w((lambda *s: torch.randn(*s, dtype=torch.bfloat16) * 0.8)(B, Hkv, Sk, Dh)),
            "v_cache": _w((lambda *s: torch.randn(*s, dtype=torch.bfloat16) * 0.05)(B, Hkv, Sk, Dh)),
            "wq": _w((lambda *s: torch.randn(*s, dtype=torch.bfloat16) * 0.8)(D, Hq * Dh) * (D ** -0.5)),
            "wo": _w((lambda *s: torch.randn(*s, dtype=torch.bfloat16) * 0.05)(Hq * Dh, D) * ((Hq * Dh) ** -0.5)),
        }
    _A_in = _A_setup()

    def _B_setup():
        B, Sq, D = 16, 48, 512
        Hq, Hkv, Sk, Dh = 32, 8, 512, 128
        assert Hq % Hkv == 0, (Hq, Hkv)
        return {
            "x": _w((lambda *s: torch.randn(*s, dtype=torch.bfloat16) * 0.8)(B, Sq, D)),
            "k_cache": _w((lambda *s: torch.randn(*s, dtype=torch.bfloat16) * 0.8)(B, Hkv, Sk, Dh)),
            "v_cache": _w((lambda *s: torch.randn(*s, dtype=torch.bfloat16) * 0.05)(B, Hkv, Sk, Dh)),
            "wq": _w((lambda *s: torch.randn(*s, dtype=torch.bfloat16) * 0.8)(D, Hq * Dh) * (D ** -0.5)),
            "wo": _w((lambda *s: torch.randn(*s, dtype=torch.bfloat16) * 0.05)(Hq * Dh, D) * ((Hq * Dh) ** -0.5)),
        }
    _B_in = _B_setup()

    return {
        "A_x": _A_in["x"],
        "A_k_cache": _A_in["k_cache"],
        "A_v_cache": _A_in["v_cache"],
        "A_wq": _A_in["wq"],
        "A_wo": _A_in["wo"],
        "B_x": _B_in["x"],
        "B_k_cache": _B_in["k_cache"],
        "B_v_cache": _B_in["v_cache"],
        "B_wq": _B_in["wq"],
        "B_wo": _B_in["wo"],
    }

def reference(A_x, A_k_cache, A_v_cache, A_wq, A_wo, B_x, B_k_cache, B_v_cache, B_wq, B_wo):
    def _A_ref(x, k_cache, v_cache, wq, wo):
        B, Sq, D = x.shape
        Hq, Dh = 32, 128
        Hkv = k_cache.shape[1]
        rep = Hq // Hkv
        q = (lambda x, *d: x.permute(*d))((x @ wq).reshape(B, Sq, Hq, Dh), 0, 2, 1, 3)
        k = (lambda x, n, axis: torch.repeat_interleave(x, n, dim=axis))(k_cache, rep, axis=1)
        v = (lambda x, n, axis: torch.repeat_interleave(x, n, dim=axis))(v_cache, rep, axis=1)
        scores = torch.einsum("bhqd,bhkd->bhqk", q, k) * (Dh ** -0.5)
        p = _softmax(scores, axis=-1)
        o = torch.einsum("bhqk,bhkd->bhqd", p, v)
        return (lambda x, *d: x.permute(*d))(o, 0, 2, 1, 3).reshape(B, Sq, Hq * Dh) @ wo

    def _B_ref(x, k_cache, v_cache, wq, wo):
        B, Sq, D = x.shape
        Hq, Dh = 32, 128
        Hkv = k_cache.shape[1]
        rep = Hq // Hkv
        q = (lambda x, *d: x.permute(*d))((x @ wq).reshape(B, Sq, Hq, Dh), 0, 2, 1, 3)
        k = (lambda x, n, axis: torch.repeat_interleave(x, n, dim=axis))(k_cache, rep, axis=1)
        v = (lambda x, n, axis: torch.repeat_interleave(x, n, dim=axis))(v_cache, rep, axis=1)
        scores = torch.einsum("bhqd,bhkd->bhqk", q, k) * (Dh ** -0.5)
        p = _softmax(scores, axis=-1)
        o = torch.einsum("bhqk,bhkd->bhqd", p, v)
        return (lambda x, *d: x.permute(*d))(o, 0, 2, 1, 3).reshape(B, Sq, Hq * Dh) @ wo

    a = _A_ref(A_x, A_k_cache, A_v_cache, A_wq, A_wo)
    b = _B_ref(B_x, B_k_cache, B_v_cache, B_wq, B_wo)
    return torch.concatenate([a, b], axis=-1)
```

```cpp
#include <hip/hip_runtime.h>
#include <hip/hip_bf16.h>
#include <stdint.h>

typedef short s16x4 __attribute__((ext_vector_type(4)));
typedef short s16x8 __attribute__((ext_vector_type(8)));
typedef __bf16 bf16x8 __attribute__((ext_vector_type(8)));
typedef __bf16 bf16x2 __attribute__((ext_vector_type(2)));
typedef float f32x4 __attribute__((ext_vector_type(4)));
typedef float f32x2 __attribute__((ext_vector_type(2)));
typedef unsigned u32x4 __attribute__((ext_vector_type(4)));
typedef unsigned u32x2 __attribute__((ext_vector_type(2)));

#define AS1 __attribute__((address_space(1)))
#define AS3 __attribute__((address_space(3)))
#define DI __device__ __forceinline__

struct Params {
  const char* x[2];
  const char* k[2];
  const char* v[2];
  const char* wq[2];
  const char* wo[2];
  char* out;
  char* ws;
};

constexpr unsigned XQ = 0;
constexpr unsigned RG = 49152;
constexpr unsigned UNIT = 16384;
constexpr int LDS_BYTES = 163840;

constexpr size_t WS_EPOCH = 0;
constexpr size_t WS_FLAG = 8192;
constexpr size_t WS_PART = 65536;

constexpr float CEXP = 0.08838834764831845f * 1.4426950408889634f;
constexpr float THR_RAW = 8.0f / CEXP;

#define WAIT_V(n) asm volatile("s_waitcnt vmcnt(" #n ")" ::: "memory")
#define WAIT_L0() asm volatile("s_waitcnt lgkmcnt(0)" ::: "memory")
#define BAR() do { asm volatile("s_waitcnt lgkmcnt(0)" ::: "memory"); __builtin_amdgcn_s_barrier(); asm volatile("" ::: "memory"); } while (0)

DI f32x4 mfma16(s16x8 a, s16x8 b, f32x4 c) {
  return __builtin_amdgcn_mfma_f32_16x16x32_bf16(__builtin_bit_cast(bf16x8, a), __builtin_bit_cast(bf16x8, b), c, 0, 0, 0);
}
DI unsigned cvtpk(float lo, float hi) {
  f32x2 v = {lo, hi};
  bf16x2 b = __builtin_convertvector(v, bf16x2);
  return __builtin_bit_cast(unsigned, b);
}
DI s16x4 trread(const char* lds, unsigned off) {
  return __builtin_amdgcn_ds_read_tr16_b64_v4i16((AS3 s16x4*)(lds + off));
}
DI s16x8 ldsr128(const char* lds, unsigned off) { return *(const s16x8*)(lds + off); }
template <int AUX> DI void dma16(__amdgpu_buffer_rsrc_t rs, unsigned voff, unsigned soff, char* lds, unsigned off) {
  __builtin_amdgcn_raw_ptr_buffer_load_lds(rs, (AS3 void*)(lds + off), 16, (int)voff, (int)soff, 0, AUX);
}
DI __amdgpu_buffer_rsrc_t mkrsrc(const void* p, unsigned bytes) {
  return __builtin_amdgcn_make_buffer_rsrc((void*)p, 0, bytes, 0x00020000);
}
DI float xmax16_32(float v) {
  unsigned u = __float_as_uint(v);
  auto r = __builtin_amdgcn_permlane16_swap(u, u, false, false);
  v = fmaxf(__uint_as_float(r[0]), __uint_as_float(r[1]));
  u = __float_as_uint(v);
  auto r2 = __builtin_amdgcn_permlane32_swap(u, u, false, false);
  return fmaxf(__uint_as_float(r2[0]), __uint_as_float(r2[1]));
}
DI float xsum16_32(float v) {
  unsigned u = __float_as_uint(v);
  auto r = __builtin_amdgcn_permlane16_swap(u, u, false, false);
  v = __uint_as_float(r[0]) + __uint_as_float(r[1]);
  u = __float_as_uint(v);
  auto r2 = __builtin_amdgcn_permlane32_swap(u, u, false, false);
  return __uint_as_float(r2[0]) + __uint_as_float(r2[1]);
}

DI constexpr unsigned kv_unit_off(int n) { return RG + (unsigned)((4 + n) % 7) * UNIT; }

__global__ __launch_bounds__(512, 2) void fused_gqa_kernel(Params p) {
  extern __shared__ __attribute__((aligned(16))) char lds[];
  const int tid = threadIdx.x, lane = tid & 63;
  const int w = __builtin_amdgcn_readfirstlane(tid >> 6);
  const int bid = blockIdx.x;
  const int kvh = bid & 7, b = (bid >> 3) & 15, br = bid >> 7;
  const int grp = bid >> 3;
  const int l15 = lane & 15, gq = lane >> 4;
  const int tq = (lane & 15) >> 2, tp = lane & 3;
  const int g = w >> 1, hh = w & 1;

  const unsigned epoch = __hip_atomic_load((const AS1 unsigned*)(p.ws + WS_EPOCH + grp * 128), __ATOMIC_RELAXED, __HIP_MEMORY_SCOPE_AGENT) + 1u;

  const __amdgpu_buffer_rsrc_t rx = mkrsrc(p.x[br] + (size_t)b * 48 * 1024, 48 * 1024);
  const __amdgpu_buffer_rsrc_t rk = mkrsrc(p.k[br] + (size_t)(b * 8 + kvh) * 131072, 131072);
  const __amdgpu_buffer_rsrc_t rv = mkrsrc(p.v[br] + (size_t)(b * 8 + kvh) * 131072, 131072);
  const __amdgpu_buffer_rsrc_t rwq = mkrsrc(p.wq[br] + kvh * 1024, 512 * 8192 - kvh * 1024);
  const __amdgpu_buffer_rsrc_t rwo = mkrsrc(p.wo[br] + (size_t)kvh * 512 * 1024, 512 * 1024);

  const int wr = lane >> 3, wpos = lane & 7;
  const unsigned wsw0 = (unsigned)(wpos ^ (((wr >> 1) & 1) << 1)) * 16 + w * 128;
  const unsigned wsw1 = wsw0 ^ 64;

  auto issue_w = [&](__amdgpu_buffer_rsrc_t rs, int lgstride, int ks, unsigned stage_off) {
    const unsigned v0 = ((unsigned)wr << lgstride) + wsw0, v1 = ((unsigned)wr << lgstride) + wsw1;
#pragma unroll
    for (int i = 0; i < 4; ++i)
      dma16<0>(rs, (i & 1) ? v1 : v0, (unsigned)(ks * 32 + i * 8) << lgstride, lds, stage_off + w * 4096 + i * 1024);
  };
  const int kr = lane >> 4, kpos = lane & 15;
  unsigned kvo[2], vvo[2];
#pragma unroll
  for (int i = 0; i < 2; ++i) {
    const int row = (2 * w + i) * 4 + kr;
    kvo[i] = (unsigned)(row * 256 + ((kpos ^ (row & 15)) * 16));
    vvo[i] = (unsigned)(row * 256 + ((kpos ^ ((row & 7) << 1)) * 16));
  }
  auto issue_kv = [&](int n) {
    const unsigned dst = RG + (unsigned)((4 + n) % 7) * UNIT + 2 * w * 1024;
    const unsigned so = (unsigned)(n >> 1) * 16384;
    if (n & 1) { dma16<2>(rv, vvo[0], so, lds, dst); dma16<2>(rv, vvo[1], so, lds, dst + 1024); }
    else       { dma16<2>(rk, kvo[0], so, lds, dst); dma16<2>(rk, kvo[1], so, lds, dst + 1024); }
  };

#pragma unroll
  for (int i = 0; i < 6; ++i) {
    const int row = w * 6 + i;
    dma16<0>(rx, (unsigned)((lane ^ (row & 15)) * 16), (unsigned)row * 1024, lds, XQ + row * 1024);
  }
  const int rot = b;
  issue_w(rwq, 13, (0 + rot) & 15, RG);
  issue_w(rwq, 13, (1 + rot) & 15, RG + 2 * UNIT);
  issue_kv(0); issue_kv(1); issue_kv(2);

  const int wf = (((tq >> 1) & 1) << 1) | ((gq & 1) << 2);
  unsigned wtr[4];
#pragma unroll
  for (int c = 0; c < 4; ++c) wtr[c] = (unsigned)((8 * gq + tq) * 128 + (((2 * c + (tp >> 1)) ^ wf) * 16) + 8 * (tp & 1)) + w * 4096;
  auto proj_step = [&](f32x4 (&acc)[4][3], unsigned stage_off, int ks) {
    s16x8 wf8[4], xf[3];
#pragma unroll
    for (int c = 0; c < 4; ++c) {
      s16x4 a0 = trread(lds, stage_off + wtr[c]);
      s16x4 a1 = trread(lds, stage_off + wtr[c] + 512);
      wf8[c] = __builtin_shufflevector(a0, a1, 0, 1, 2, 3, 4, 5, 6, 7);
    }
#pragma unroll
    for (int rt = 0; rt < 3; ++rt) xf[rt] = ldsr128(lds, XQ + (unsigned)((16 * rt + l15) * 1024 + (((4 * ks + gq) ^ l15) * 16)));
    asm volatile("s_waitcnt lgkmcnt(0)" : "+v"(wf8[0]), "+v"(wf8[1]), "+v"(wf8[2]), "+v"(wf8[3]), "+v"(xf[0]), "+v"(xf[1]), "+v"(xf[2]) :: "memory");
    return [=](f32x4 (&acc2)[4][3]) {
#pragma unroll
      for (int c = 0; c < 4; ++c)
#pragma unroll
        for (int rt = 0; rt < 3; ++rt) acc2[c][rt] = mfma16(wf8[c], xf[rt], acc2[c][rt]);
    };
  };

  f32x4 acc[4][3];
#pragma unroll
  for (int c = 0; c < 4; ++c)
#pragma unroll
    for (int rt = 0; rt < 3; ++rt) acc[c][rt] = f32x4{0.f, 0.f, 0.f, 0.f};

  WAIT_V(10); BAR();
  {
    auto mm = proj_step(acc, RG, (0 + rot) & 15);
    issue_w(rwq, 13, (2 + rot) & 15, RG);
    mm(acc);
    WAIT_V(10);
    auto mm2 = proj_step(acc, RG + 2 * UNIT, (1 + rot) & 15);
    issue_w(rwq, 13, (3 + rot) & 15, RG + 2 * UNIT);
    mm2(acc);
  }
#pragma unroll 1
  for (int i = 2; i < 14; i += 2) {
    WAIT_V(4);
    auto mm = proj_step(acc, RG, (i + rot) & 15);
    issue_w(rwq, 13, (i + 2 + rot) & 15, RG);
    mm(acc);
    WAIT_V(4);
    auto mm2 = proj_step(acc, RG + 2 * UNIT, (i + 1 + rot) & 15);
    issue_w(rwq, 13, (i + 3 + rot) & 15, RG + 2 * UNIT);
    mm2(acc);
  }
  {
    WAIT_V(4);
    auto mm = proj_step(acc, RG, (14 + rot) & 15);
    mm(acc);
    WAIT_V(0);
    auto mm2 = proj_step(acc, RG + 2 * UNIT, (15 + rot) & 15);
    mm2(acc);
  }
  BAR();
  issue_kv(3); issue_kv(4); issue_kv(5); issue_kv(6);
#pragma unroll
  for (int c = 0; c < 4; ++c)
#pragma unroll
    for (int rt = 0; rt < 3; ++rt) {
      const int row = 16 * rt + l15;
      const int chunk = 8 * hh + 2 * c + (gq >> 1);
      u32x2 v = {cvtpk(acc[c][rt][0], acc[c][rt][1]), cvtpk(acc[c][rt][2], acc[c][rt][3])};
      *(u32x2*)(lds + XQ + g * 12288 + row * 256 + ((chunk ^ l15) * 16) + (gq & 1) * 8) = v;
    }
  BAR();
  s16x8 qf[3][4];
#pragma unroll
  for (int rt = 0; rt < 3; ++rt)
#pragma unroll
    for (int s = 0; s < 4; ++s) qf[rt][s] = ldsr128(lds, XQ + g * 12288 + (16 * rt + l15) * 256 + (((4 * s + gq) ^ l15) * 16));

  f32x4 o[8][3];
#pragma unroll
  for (int c = 0; c < 8; ++c)
#pragma unroll
    for (int rt = 0; rt < 3; ++rt) o[c][rt] = f32x4{0.f, 0.f, 0.f, 0.f};
  float mrow[3], lrow[3];
#pragma unroll
  for (int rt = 0; rt < 3; ++rt) { mrow[rt] = 0.f; lrow[rt] = 0.f; }
  const unsigned koff = (unsigned)((32 * hh + l15) * 256);
  const int vrow = 32 * hh + 4 * gq + tq;
  const int vsw = (vrow & 7) << 1;

#pragma unroll 1
  for (int t = 0; t < 8; ++t) {
    if (t == 0) WAIT_V(10);
    else if (t <= 5) WAIT_V(6);
    else WAIT_V(4);
    BAR();
    if (t >= 1 && t <= 4) { issue_kv(2 * t + 5); issue_kv(2 * t + 6); }
    else if (t == 5) issue_kv(15);
    else if (t == 6) issue_w(rwo, 10, (0 + rot) & 15, RG);
    else if (t == 7) issue_w(rwo, 10, (1 + rot) & 15, RG + 2 * UNIT);

    const unsigned ku = RG + (unsigned)((4 + 2 * t) % 7) * UNIT, vu = RG + (unsigned)((5 + 2 * t) % 7) * UNIT;
    f32x4 st[2][3];
#pragma unroll
    for (int kt = 0; kt < 2; ++kt)
#pragma unroll
      for (int rt = 0; rt < 3; ++rt) st[kt][rt] = f32x4{0.f, 0.f, 0.f, 0.f};
#pragma unroll
    for (int s = 0; s < 4; ++s)
#pragma unroll
      for (int kt = 0; kt < 2; ++kt) {
        s16x8 kf = ldsr128(lds, ku + koff + kt * 4096 + (((4 * s + gq) ^ l15) * 16));
#pragma unroll
        for (int rt = 0; rt < 3; ++rt) st[kt][rt] = mfma16(kf, qf[rt][s], st[kt][rt]);
      }
    float mx[3];
#pragma unroll
    for (int rt = 0; rt < 3; ++rt) {
      float a = fmaxf(fmaxf(st[0][rt][0], st[0][rt][1]), fmaxf(st[0][rt][2], st[0][rt][3]));
      float c2 = fmaxf(fmaxf(st[1][rt][0], st[1][rt][1]), fmaxf(st[1][rt][2], st[1][rt][3]));
      mx[rt] = xmax16_32(fmaxf(a, c2));
    }
    if (t == 0) {
#pragma unroll
      for (int rt = 0; rt < 3; ++rt) mrow[rt] = mx[rt];
    } else {
      const bool grow = (mx[0] - mrow[0] > THR_RAW) | (mx[1] - mrow[1] > THR_RAW) | (mx[2] - mrow[2] > THR_RAW);
      if (__any(grow)) {
#pragma unroll
        for (int rt = 0; rt < 3; ++rt) {
          const float mn = fmaxf(mrow[rt], mx[rt]);
          const float al = __builtin_amdgcn_exp2f((mrow[rt] - mn) * CEXP);
          mrow[rt] = mn; lrow[rt] *= al;
#pragma unroll
          for (int c = 0; c < 8; ++c) o[c][rt] *= al;
        }
      }
    }
    s16x8 pf[3];
#pragma unroll
    for (int rt = 0; rt < 3; ++rt) {
      const float nm = -mrow[rt] * CEXP;
      float e[8];
#pragma unroll
      for (int j = 0; j < 4; ++j) { e[j] = __builtin_amdgcn_exp2f(fmaf(st[0][rt][j], CEXP, nm)); e[4 + j] = __builtin_amdgcn_exp2f(fmaf(st[1][rt][j], CEXP, nm)); }
      lrow[rt] += ((e[0] + e[1]) + (e[2] + e[3])) + ((e[4] + e[5]) + (e[6] + e[7]));
      u32x4 pk = {cvtpk(e[0], e[1]), cvtpk(e[2], e[3]), cvtpk(e[4], e[5]), cvtpk(e[6], e[7])};
      pf[rt] = __builtin_bit_cast(s16x8, pk);
    }
#pragma unroll
    for (int c = 0; c < 8; ++c) {
      const unsigned a = vu + (unsigned)(vrow * 256 + (((2 * c + (tp >> 1)) ^ vsw) * 16) + 8 * (tp & 1));
      s16x4 v0 = trread(lds, a);
      s16x4 v1 = trread(lds, a + 4096);
      s16x8 vf = __builtin_shufflevector(v0, v1, 0, 1, 2, 3, 4, 5, 6, 7);
#pragma unroll
      for (int rt = 0; rt < 3; ++rt) o[c][rt] = mfma16(vf, pf[rt], o[c][rt]);
    }
  }

#pragma unroll
  for (int rt = 0; rt < 3; ++rt) lrow[rt] = xsum16_32(lrow[rt]);
  {
    float* stat = (float*)(lds + XQ);
    if (gq == 0) {
#pragma unroll
      for (int rt = 0; rt < 3; ++rt) { stat[((w * 3 + rt) * 16 + l15) * 2] = mrow[rt]; stat[((w * 3 + rt) * 16 + l15) * 2 + 1] = lrow[rt]; }
    }
    BAR();
    float sc[3];
#pragma unroll
    for (int rt = 0; rt < 3; ++rt) {
      const float m2 = stat[(((w ^ 1) * 3 + rt) * 16 + l15) * 2], l2 = stat[(((w ^ 1) * 3 + rt) * 16 + l15) * 2 + 1];
      const float mn = fmaxf(mrow[rt], m2);
      const float w1 = __builtin_amdgcn_exp2f((mrow[rt] - mn) * CEXP), w2 = __builtin_amdgcn_exp2f((m2 - mn) * CEXP);
      sc[rt] = w1 / (lrow[rt] * w1 + l2 * w2);
    }
#pragma unroll
    for (int c = 0; c < 8; ++c)
#pragma unroll
      for (int rt = 0; rt < 3; ++rt) o[c][rt] *= sc[rt];
    BAR();
  }
#pragma unroll
  for (int r = 0; r < 2; ++r) {
#pragma unroll
    for (int i = 0; i < 6; ++i) {
      const int idx = r * 6 + i, cc = idx / 3, rt = idx % 3;
      const f32x4 v = hh ? o[cc][rt] : o[4 + cc][rt];
      *(f32x4*)(lds + XQ + w * 6144 + i * 1024 + lane * 16) = v;
    }
    BAR();
#pragma unroll
    for (int i = 0; i < 6; ++i) {
      const int idx = r * 6 + i, cc = idx / 3, rt = idx % 3;
      const f32x4 v = *(const f32x4*)(lds + XQ + (w ^ 1) * 6144 + i * 1024 + lane * 16);
      if (hh) o[4 + cc][rt] += v; else o[cc][rt] += v;
    }
    BAR();
  }
#pragma unroll
  for (int cc = 0; cc < 4; ++cc)
#pragma unroll
    for (int rt = 0; rt < 3; ++rt) {
      const f32x4 v = hh ? o[4 + cc][rt] : o[cc][rt];
      const int row = 16 * rt + l15;
      const int chunk = g * 16 + 8 * hh + 2 * cc + (gq >> 1);
      u32x2 pk = {cvtpk(v[0], v[1]), cvtpk(v[2], v[3])};
      *(u32x2*)(lds + XQ + row * 1024 + ((chunk ^ l15) * 16) + (gq & 1) * 8) = pk;
    }
  BAR();

#pragma unroll
  for (int c = 0; c < 4; ++c)
#pragma unroll
    for (int rt = 0; rt < 3; ++rt) acc[c][rt] = f32x4{0.f, 0.f, 0.f, 0.f};
#pragma unroll 1
  for (int i = 0; i < 14; i += 2) {
    WAIT_V(4);
    auto mm = proj_step(acc, RG, (i + rot) & 15);
    issue_w(rwo, 10, (i + 2 + rot) & 15, RG);
    mm(acc);
    WAIT_V(4);
    auto mm2 = proj_step(acc, RG + 2 * UNIT, (i + 1 + rot) & 15);
    issue_w(rwo, 10, (i + 3 + rot) & 15, RG + 2 * UNIT);
    mm2(acc);
  }
  {
    WAIT_V(4);
    auto mm = proj_step(acc, RG, (14 + rot) & 15);
    mm(acc);
    WAIT_V(0);
    auto mm2 = proj_step(acc, RG + 2 * UNIT, (15 + rot) & 15);
    mm2(acc);
  }

  {
    char* part = p.ws + WS_PART;
    const __amdgpu_buffer_rsrc_t rs = __builtin_amdgcn_make_buffer_rsrc(part + (size_t)grp * 8 * 8 * 12288, 0, 8 * 8 * 12288, 0x00020000);
    const unsigned base = (unsigned)((w * 8 + kvh) * 12288);
#pragma unroll
    for (int c = 0; c < 4; ++c)
#pragma unroll
      for (int rt = 0; rt < 3; ++rt) {
        const unsigned off = base + (unsigned)((16 * rt + l15) * 256 + (16 * c + 4 * gq) * 4);
        __builtin_amdgcn_raw_buffer_store_b128(__builtin_bit_cast(u32x4, acc[c][rt]), rs, off, 0, 16);
      }
    WAIT_V(0);
    BAR();
    AS1 unsigned* flags = (AS1 unsigned*)(p.ws + WS_FLAG + (size_t)grp * 8 * 128);
    if (tid == 0) __hip_atomic_store(flags + kvh * 32, epoch, __ATOMIC_RELAXED, __HIP_MEMORY_SCOPE_AGENT);
    if (w == 0) {
      for (unsigned spins = 0; spins < 4000000u; ++spins) {
        unsigned f = epoch;
        if (lane < 8) f = __hip_atomic_load(flags + lane * 32, __ATOMIC_RELAXED, __HIP_MEMORY_SCOPE_AGENT);
        if (__all(f == epoch)) break;
        __builtin_amdgcn_s_sleep(1);
      }
      __builtin_amdgcn_fence(__ATOMIC_ACQUIRE, "agent");
      WAIT_V(0);
    }
    BAR();
    if (tid == 0) __hip_atomic_store((AS1 unsigned*)(p.ws + WS_EPOCH + grp * 128), epoch, __ATOMIC_RELAXED, __HIP_MEMORY_SCOPE_AGENT);
    const char* src = part + (size_t)grp * 8 * 8 * 12288 + (size_t)kvh * 8 * 12288;
    char* outp = p.out + (size_t)b * 48 * 2048 + br * 1024 + kvh * 128;
#pragma unroll
    for (int rep = 0; rep < 2; ++rep) {
      const int q = tid + rep * 512;
      if (q < 768) {
        f32x4 s = {0.f, 0.f, 0.f, 0.f};
        f32x4 v[8];
#pragma unroll
        for (int sidx = 0; sidx < 8; ++sidx) v[sidx] = *(const f32x4*)(src + sidx * 12288 + q * 16);
#pragma unroll
        for (int sidx = 0; sidx < 8; ++sidx) s += v[sidx];
        const int row = q >> 4, c4 = q & 15;
        u32x2 pk = {cvtpk(s[0], s[1]), cvtpk(s[2], s[3])};
        *(u32x2*)(outp + (size_t)row * 2048 + c4 * 8) = pk;
      }
    }
  }
}

extern "C" void kernel_launch(void* const* d_in, const int* in_sizes, int n_in, void* d_out, int out_size, void* d_ws, size_t ws_size,
                              hipStream_t stream) {
  Params p;
  for (int br = 0; br < 2; ++br) {
    p.x[br] = (const char*)d_in[5 * br + 0];
    p.k[br] = (const char*)d_in[5 * br + 1];
    p.v[br] = (const char*)d_in[5 * br + 2];
    p.wq[br] = (const char*)d_in[5 * br + 3];
    p.wo[br] = (const char*)d_in[5 * br + 4];
  }
  p.out = (char*)d_out;
  p.ws = (char*)d_ws;
  (void)hipFuncSetAttribute((const void*)fused_gqa_kernel, hipFuncAttributeMaxDynamicSharedMemorySize, LDS_BYTES);
  fused_gqa_kernel<<<256, 512, LDS_BYTES, stream>>>(p);
}
```

```cpp
#include <hip/hip_runtime.h>
#include <hip/hip_bf16.h>
#include <stdint.h>

typedef short s16x4 __attribute__((ext_vector_type(4)));
typedef short s16x8 __attribute__((ext_vector_type(8)));
typedef __bf16 bf16x8 __attribute__((ext_vector_type(8)));
typedef __bf16 bf16x2 __attribute__((ext_vector_type(2)));
typedef float f32x4 __attribute__((ext_vector_type(4)));
typedef float f32x2 __attribute__((ext_vector_type(2)));
typedef unsigned u32x4 __attribute__((ext_vector_type(4)));
typedef unsigned u32x2 __attribute__((ext_vector_type(2)));

#define AS1 __attribute__((address_space(1)))
#define AS3 __attribute__((address_space(3)))
#define DI __device__ __forceinline__

struct Params {
  const char* x[2];
  const char* k[2];
  const char* v[2];
  const char* wq[2];
  const char* wo[2];
  char* out;
  char* ws;
};

constexpr unsigned XQ = 0;
constexpr unsigned RG = 49152;
constexpr unsigned UNIT = 16384;
constexpr int LDS_BYTES = 163840;

constexpr size_t WS_EPOCH = 0;
constexpr size_t WS_FLAG = 8192;
constexpr size_t WS_PART = 65536;

constexpr float CEXP = 0.08838834764831845f * 1.4426950408889634f;
constexpr float THR_RAW = 8.0f / CEXP;

#define WAIT_V(n) asm volatile("s_waitcnt vmcnt(" #n ")" ::: "memory")
#define BAR() do { asm volatile("s_waitcnt lgkmcnt(0)" ::: "memory"); __builtin_amdgcn_s_barrier(); asm volatile("" ::: "memory"); } while (0)
#define STAMP(i) do {} while (0)

DI f32x4 mfma16(s16x8 a, s16x8 b, f32x4 c) {
  return __builtin_amdgcn_mfma_f32_16x16x32_bf16(__builtin_bit_cast(bf16x8, a), __builtin_bit_cast(bf16x8, b), c, 0, 0, 0);
}
DI unsigned cvtpk(float lo, float hi) {
  f32x2 v = {lo, hi};
  bf16x2 b = __builtin_convertvector(v, bf16x2);
  return __builtin_bit_cast(unsigned, b);
}
DI s16x4 trread(const char* lds, unsigned off) {
  return __builtin_amdgcn_ds_read_tr16_b64_v4i16((AS3 s16x4*)(lds + off));
}
DI s16x8 trread2(const char* lds, unsigned off0, unsigned off1) {
  s16x4 a = trread(lds, off0), b = trread(lds, off1);
  return __builtin_shufflevector(a, b, 0, 1, 2, 3, 4, 5, 6, 7);
}
DI s16x8 ldsr128(const char* lds, unsigned off) { return *(const s16x8*)(lds + off); }
DI void dma_d(__amdgpu_buffer_rsrc_t rs, unsigned voff, unsigned soff, unsigned ldsaddr) {
  asm volatile("s_mov_b32 m0, %0\n\ts_nop 0\n\tbuffer_load_dwordx4 %1, %2, %3 offen lds" ::"s"(ldsaddr), "v"(voff), "s"(rs), "s"(soff) : "memory");
}
DI void dma_nt(__amdgpu_buffer_rsrc_t rs, unsigned voff, unsigned soff, unsigned ldsaddr) {
  asm volatile("s_mov_b32 m0, %0\n\ts_nop 0\n\tbuffer_load_dwordx4 %1, %2, %3 offen nt lds" ::"s"(ldsaddr), "v"(voff), "s"(rs), "s"(soff) : "memory");
}
DI __amdgpu_buffer_rsrc_t mkrsrc(const void* p, unsigned bytes) {
  return __builtin_amdgcn_make_buffer_rsrc((void*)p, 0, bytes, 0x00020000);
}
DI float xmax16_32(float v) {
  unsigned u = __float_as_uint(v);
  auto r = __builtin_amdgcn_permlane16_swap(u, u, false, false);
  v = fmaxf(__uint_as_float(r[0]), __uint_as_float(r[1]));
  u = __float_as_uint(v);
  auto r2 = __builtin_amdgcn_permlane32_swap(u, u, false, false);
  return fmaxf(__uint_as_float(r2[0]), __uint_as_float(r2[1]));
}
DI float xsum16_32(float v) {
  unsigned u = __float_as_uint(v);
  auto r = __builtin_amdgcn_permlane16_swap(u, u, false, false);
  v = __uint_as_float(r[0]) + __uint_as_float(r[1]);
  u = __float_as_uint(v);
  auto r2 = __builtin_amdgcn_permlane32_swap(u, u, false, false);
  return __uint_as_float(r2[0]) + __uint_as_float(r2[1]);
}

__global__ __launch_bounds__(512, 2) void fused_gqa_kernel(Params p) {
  extern __shared__ __attribute__((aligned(16))) char lds[];
  const int tid = threadIdx.x, lane = tid & 63;
  const int w = __builtin_amdgcn_readfirstlane(tid >> 6);
  const int bid = blockIdx.x;
  const int kvh = bid & 7, b = (bid >> 3) & 15, br = bid >> 7;
  const int grp = bid >> 3;
  const int l15 = lane & 15, gq = lane >> 4;
  const int tq = (lane & 15) >> 2, tp = lane & 3;
  const int g = w >> 1, hh = w & 1;
  const unsigned ldsb = (unsigned)(uintptr_t)lds;

  STAMP(0);
  const __amdgpu_buffer_rsrc_t rx = mkrsrc(p.x[br] + (size_t)b * 48 * 1024, 48 * 1024);
  const __amdgpu_buffer_rsrc_t rk = mkrsrc(p.k[br] + (size_t)(b * 8 + kvh) * 131072, 131072);
  const __amdgpu_buffer_rsrc_t rv = mkrsrc(p.v[br] + (size_t)(b * 8 + kvh) * 131072, 131072);
  const __amdgpu_buffer_rsrc_t rwq = mkrsrc(p.wq[br] + kvh * 1024, 512 * 8192 - kvh * 1024);
  const __amdgpu_buffer_rsrc_t rwo = mkrsrc(p.wo[br] + (size_t)kvh * 512 * 1024, 512 * 1024);

  const int wr = lane >> 3, wpos = lane & 7;
  const unsigned wsw0 = (unsigned)(wpos ^ (((wr >> 1) & 1) << 1)) * 16 + w * 128;

  unsigned wv0 = ((unsigned)wr << 13) + wsw0;
  auto issue_w = [&](__amdgpu_buffer_rsrc_t rs, int lgstride, int ks, unsigned stage_off) {
    const unsigned v0 = wv0, v1 = wv0 ^ 64;
#pragma unroll
    for (int i = 0; i < 4; ++i)
      dma_d(rs, (i & 1) ? v1 : v0, (unsigned)(ks * 32 + i * 8) << lgstride, ldsb + stage_off + w * 4096 + i * 1024);
  };
  const int kr = lane >> 4, kpos = lane & 15;
  unsigned kvo[2], vvo[2];
#pragma unroll
  for (int i = 0; i < 2; ++i) {
    const int row = (2 * w + i) * 4 + kr;
    kvo[i] = (unsigned)(row * 256 + ((kpos ^ (row & 15)) * 16));
    vvo[i] = (unsigned)(row * 256 + ((kpos ^ ((row & 7) << 1)) * 16));
  }
  auto issue_kv = [&](int n) {
    const unsigned dst = ldsb + RG + (unsigned)((4 + n) % 7) * UNIT + 2 * w * 1024;
    const unsigned so = (unsigned)(n >> 1) * 16384;
    if (n & 1) { dma_nt(rv, vvo[0], so, dst); dma_nt(rv, vvo[1], so, dst + 1024); }
    else       { dma_nt(rk, kvo[0], so, dst); dma_nt(rk, kvo[1], so, dst + 1024); }
  };

  const int rot = b;
#pragma unroll
  for (int i = 0; i < 6; ++i) {
    const int row = w * 6 + i;
    dma_d(rx, (unsigned)((lane ^ (row & 15)) * 16), (unsigned)row * 1024, ldsb + XQ + row * 1024);
  }
  issue_w(rwq, 13, (0 + rot) & 15, RG);
  issue_w(rwq, 13, (1 + rot) & 15, RG + 2 * UNIT);
  __builtin_amdgcn_s_barrier();
  issue_kv(0); issue_kv(1); issue_kv(2);
  const unsigned epoch = __hip_atomic_load((const AS1 unsigned*)(p.ws + WS_EPOCH + grp * 128), __ATOMIC_RELAXED, __HIP_MEMORY_SCOPE_AGENT) + 1u;

  unsigned wtr[4];
  auto calc_wtr = [&](int ln) {
    const int gq_ = ln >> 4, tq_ = (ln & 15) >> 2, tp_ = ln & 3;
    const int wf = (((tq_ >> 1) & 1) << 1) | ((gq_ & 1) << 2);
#pragma unroll
    for (int c = 0; c < 4; ++c) wtr[c] = (unsigned)((8 * gq_ + tq_) * 128 + (((2 * c + (tp_ >> 1)) ^ wf) * 16) + 8 * (tp_ & 1)) + w * 4096;
  };
  calc_wtr(lane);

  struct Frag { s16x8 w[4]; s16x8 x[3]; };
  auto rd_frag1 = [&](Frag& f, unsigned stage_off, int ks) {
#pragma unroll
    for (int c = 0; c < 4; ++c) f.w[c] = trread2(lds, stage_off + wtr[c], stage_off + wtr[c] + 512);
#pragma unroll
    for (int rt = 0; rt < 3; ++rt) f.x[rt] = ldsr128(lds, XQ + (unsigned)((16 * rt + l15) * 1024 + (((4 * ks + gq) ^ l15) * 16)));
  };
  auto pin = [&](Frag& f) {
    asm volatile("s_waitcnt lgkmcnt(0)" : "+v"(f.w[0]), "+v"(f.w[1]), "+v"(f.w[2]), "+v"(f.w[3]), "+v"(f.x[0]), "+v"(f.x[1]), "+v"(f.x[2])::"memory");
  };
  f32x4 acc[4][3];
  auto mm = [&](Frag& f) {
#pragma unroll
    for (int c = 0; c < 4; ++c)
#pragma unroll
      for (int rt = 0; rt < 3; ++rt) acc[c][rt] = mfma16(f.w[c], f.x[rt], acc[c][rt]);
  };

#pragma unroll
  for (int c = 0; c < 4; ++c)
#pragma unroll
    for (int rt = 0; rt < 3; ++rt) acc[c][rt] = f32x4{0.f, 0.f, 0.f, 0.f};
  constexpr unsigned S0 = RG, S1 = RG + 2 * UNIT;
  Frag fa, fb;
  WAIT_V(11); BAR();
  STAMP(1);
  rd_frag1(fa, S0, (0 + rot) & 15); pin(fa);
  issue_w(rwq, 13, (2 + rot) & 15, S0);
  WAIT_V(11);
  rd_frag1(fb, S1, (1 + rot) & 15);
  mm(fa);
  pin(fb);
  issue_w(rwq, 13, (3 + rot) & 15, S1);
#pragma unroll 1
  for (int i = 2; i < 14; i += 2) {
    WAIT_V(4);
    rd_frag1(fa, S0, (i + rot) & 15);
    mm(fb);
    pin(fa);
    issue_w(rwq, 13, (i + 2 + rot) & 15, S0);
    WAIT_V(4);
    rd_frag1(fb, S1, (i + 1 + rot) & 15);
    mm(fa);
    pin(fb);
    issue_w(rwq, 13, (i + 3 + rot) & 15, S1);
  }
  WAIT_V(4);
  rd_frag1(fa, S0, (14 + rot) & 15);
  mm(fb);
  pin(fa);
  WAIT_V(0);
  rd_frag1(fb, S1, (15 + rot) & 15);
  mm(fa);
  pin(fb);
  BAR();
  STAMP(2);
  issue_kv(3); issue_kv(4); issue_kv(5); issue_kv(6);
  mm(fb);
#pragma unroll
  for (int c = 0; c < 4; ++c)
#pragma unroll
    for (int rt = 0; rt < 3; ++rt) {
      const int row = 16 * rt + l15;
      const int chunk = 8 * hh + 2 * c + (gq >> 1);
      u32x2 v = {cvtpk(acc[c][rt][0], acc[c][rt][1]), cvtpk(acc[c][rt][2], acc[c][rt][3])};
      *(u32x2*)(lds + XQ + g * 12288 + row * 256 + ((chunk ^ l15) * 16) + (gq & 1) * 8) = v;
    }
  WAIT_V(12);
  BAR();
  s16x8 qf[3][4];
#pragma unroll
  for (int rt = 0; rt < 3; ++rt)
#pragma unroll
    for (int s = 0; s < 4; ++s) qf[rt][s] = ldsr128(lds, XQ + g * 12288 + (16 * rt + l15) * 256 + (((4 * s + gq) ^ l15) * 16));

  wv0 = (wv0 & 8191u) + ((wv0 >> 13) << 10);
  STAMP(3);
  f32x4 o[8][3];
#pragma unroll
  for (int c = 0; c < 8; ++c)
#pragma unroll
    for (int rt = 0; rt < 3; ++rt) o[c][rt] = f32x4{0.f, 0.f, 0.f, 0.f};
  float mrow[3], lrow[3];
#pragma unroll
  for (int rt = 0; rt < 3; ++rt) { mrow[rt] = 0.f; lrow[rt] = 0.f; }
  const unsigned koff = (unsigned)((32 * hh + l15) * 256 + ((gq ^ l15) * 16));
  const int vrow = 32 * hh + 4 * gq + tq;
  const int vsw = (vrow & 7) << 1;
  const unsigned voff = (unsigned)(vrow * 256 + (((tp >> 1) ^ vsw) * 16) + 8 * (tp & 1));

  typedef f32x4 Stile[2][3];
  auto qk = [&](Stile& st, unsigned ku) {
#pragma unroll
    for (int kt = 0; kt < 2; ++kt)
#pragma unroll
      for (int rt = 0; rt < 3; ++rt) st[kt][rt] = f32x4{0.f, 0.f, 0.f, 0.f};
    unsigned kb = ku + koff; asm volatile("" : "+v"(kb));
#pragma unroll
    for (int s = 0; s < 4; ++s)
#pragma unroll
      for (int kt = 0; kt < 2; ++kt) {
        s16x8 kf = ldsr128(lds, (kb ^ (unsigned)(s << 6)) + kt * 4096);
#pragma unroll
        for (int rt = 0; rt < 3; ++rt) st[kt][rt] = mfma16(kf, qf[rt][s], st[kt][rt]);
      }
  };
  auto softmax = [&](Stile& st, s16x8 (&pf)[3], bool first) {
    float mx[3];
#pragma unroll
    for (int rt = 0; rt < 3; ++rt) {
      float a = fmaxf(fmaxf(st[0][rt][0], st[0][rt][1]), fmaxf(st[0][rt][2], st[0][rt][3]));
      float c2 = fmaxf(fmaxf(st[1][rt][0], st[1][rt][1]), fmaxf(st[1][rt][2], st[1][rt][3]));
      mx[rt] = xmax16_32(fmaxf(a, c2));
    }
    if (first) {
#pragma unroll
      for (int rt = 0; rt < 3; ++rt) mrow[rt] = mx[rt];
    } else {
      const bool grow = (mx[0] - mrow[0] > THR_RAW) | (mx[1] - mrow[1] > THR_RAW) | (mx[2] - mrow[2] > THR_RAW);
      if (__any(grow)) {
#pragma unroll
        for (int rt = 0; rt < 3; ++rt) {
          const float mn = fmaxf(mrow[rt], mx[rt]);
          const float al = __builtin_amdgcn_exp2f((mrow[rt] - mn) * CEXP);
          mrow[rt] = mn; lrow[rt] *= al;
#pragma unroll
          for (int c = 0; c < 8; ++c) o[c][rt] *= al;
        }
      }
    }
#pragma unroll
    for (int rt = 0; rt < 3; ++rt) {
      const float nm = -mrow[rt] * CEXP;
      float e[8];
#pragma unroll
      for (int j = 0; j < 4; ++j) { e[j] = __builtin_amdgcn_exp2f(fmaf(st[0][rt][j], CEXP, nm)); e[4 + j] = __builtin_amdgcn_exp2f(fmaf(st[1][rt][j], CEXP, nm)); }
      lrow[rt] += ((e[0] + e[1]) + (e[2] + e[3])) + ((e[4] + e[5]) + (e[6] + e[7]));
      u32x4 pk = {cvtpk(e[0], e[1]), cvtpk(e[2], e[3]), cvtpk(e[4], e[5]), cvtpk(e[6], e[7])};
      pf[rt] = __builtin_bit_cast(s16x8, pk);
    }
  };
  auto pv = [&](s16x8 (&pf)[3], unsigned vu) {
    unsigned vb = vu + voff; asm volatile("" : "+v"(vb));
#pragma unroll
    for (int c = 0; c < 8; ++c) {
      const unsigned a = vb ^ (unsigned)(c << 5);
      s16x8 vf = trread2(lds, a, a + 4096);
#pragma unroll
      for (int rt = 0; rt < 3; ++rt) o[c][rt] = mfma16(vf, pf[rt], o[c][rt]);
    }
  };
  auto uoff = [&](int n) { return RG + (unsigned)((4 + n) % 7) * UNIT; };
  auto step_top = [&](int t) {
    if (t == 0) WAIT_V(8);
    else if (t <= 5) WAIT_V(6);
    else if (t == 6) WAIT_V(2);
    else WAIT_V(4);
    BAR();
    if (t == 0) issue_kv(7);
    else if (t <= 4) { issue_kv(2 * t + 6); issue_kv(2 * t + 7); }
    else if (t == 6) issue_w(rwo, 10, (0 + rot) & 15, S0);
    else if (t == 7) issue_w(rwo, 10, (1 + rot) & 15, S1);
  };

  Stile sa, sb;
  s16x8 pf[3];
  qk(sa, uoff(0));
#pragma unroll 1
  for (int t = 0; t < 8; t += 2) {
    step_top(t);
    qk(sb, uoff(2 * t + 2));
    softmax(sa, pf, t == 0);
    pv(pf, uoff(2 * t + 1));
    step_top(t + 1);
    if (t + 2 < 8) qk(sa, uoff(2 * t + 4));
    softmax(sb, pf, false);
    pv(pf, uoff(2 * t + 3));
  }

  STAMP(4);
#pragma unroll
  for (int rt = 0; rt < 3; ++rt) lrow[rt] = xsum16_32(lrow[rt]);
  const unsigned eoff_me = (w < 4) ? XQ + w * 12288 : RG + 4 * UNIT + (w - 4) * 12288;
  const unsigned eoff_pt = ((w ^ 1) < 4) ? XQ + (w ^ 1) * 12288 : RG + 4 * UNIT + ((w ^ 1) - 4) * 12288;
  {
    float* stat = (float*)(lds + XQ);
    if (gq == 0) {
#pragma unroll
      for (int rt = 0; rt < 3; ++rt) { stat[((w * 3 + rt) * 16 + l15) * 2] = mrow[rt]; stat[((w * 3 + rt) * 16 + l15) * 2 + 1] = lrow[rt]; }
    }
    BAR();
    float sc[3];
#pragma unroll
    for (int rt = 0; rt < 3; ++rt) {
      const float m2 = stat[(((w ^ 1) * 3 + rt) * 16 + l15) * 2], l2 = stat[(((w ^ 1) * 3 + rt) * 16 + l15) * 2 + 1];
      const float mn = fmaxf(mrow[rt], m2);
      const float w1 = __builtin_amdgcn_exp2f((mrow[rt] - mn) * CEXP), w2 = __builtin_amdgcn_exp2f((m2 - mn) * CEXP);
      sc[rt] = w1 / (lrow[rt] * w1 + l2 * w2);
    }
    BAR();
#pragma unroll
    for (int cc = 0; cc < 4; ++cc)
#pragma unroll
      for (int rt = 0; rt < 3; ++rt) {
        o[cc][rt] *= sc[rt]; o[4 + cc][rt] *= sc[rt];
        const f32x4 v = hh ? o[cc][rt] : o[4 + cc][rt];
        *(f32x4*)(lds + eoff_me + (cc * 3 + rt) * 1024 + lane * 16) = v;
      }
    BAR();
#pragma unroll
    for (int cc = 0; cc < 4; ++cc)
#pragma unroll
      for (int rt = 0; rt < 3; ++rt) {
        const f32x4 r = *(const f32x4*)(lds + eoff_pt + (cc * 3 + rt) * 1024 + lane * 16);
        const f32x4 v = (hh ? o[4 + cc][rt] : o[cc][rt]) + r;
        o[cc][rt] = v;
      }
    asm volatile("s_waitcnt lgkmcnt(0)" ::: "memory");
#pragma unroll
    for (int cc = 0; cc < 4; ++cc)
#pragma unroll
      for (int rt = 0; rt < 3; ++rt) {
        const f32x4 v = o[cc][rt];
        const int row = 16 * rt + l15;
        const int chunk = 2 * cc + (gq >> 1);
        u32x2 pk = {cvtpk(v[0], v[1]), cvtpk(v[2], v[3])};
        *(u32x2*)(lds + eoff_pt + row * 128 + ((chunk ^ ((l15 >> 1) & 7)) * 16) + (gq & 1) * 8) = pk;
      }
    BAR();
  }

  STAMP(5);
  int lane3 = lane; asm volatile("" : "+v"(lane3));
  calc_wtr(lane3);
  const int l15c = lane3 & 15, gqc = lane3 >> 4;
  auto rd_frag3 = [&](Frag& f, unsigned stage_off, int ks) {
#pragma unroll
    for (int c = 0; c < 4; ++c) f.w[c] = trread2(lds, stage_off + wtr[c], stage_off + wtr[c] + 512);
    const int wb = (ks >> 1) ^ 1;
    const unsigned blk = (wb < 4) ? XQ + wb * 12288 : RG + 4 * UNIT + (wb - 4) * 12288;
#pragma unroll
    for (int rt = 0; rt < 3; ++rt) f.x[rt] = ldsr128(lds, blk + (unsigned)((16 * rt + l15c) * 128 + (((4 * (ks & 1) + gqc) ^ ((l15c >> 1) & 7)) * 16)));
  };
#pragma unroll
  for (int c = 0; c < 4; ++c)
#pragma unroll
    for (int rt = 0; rt < 3; ++rt) acc[c][rt] = f32x4{0.f, 0.f, 0.f, 0.f};
  WAIT_V(4);
  rd_frag3(fa, S0, (0 + rot) & 15); pin(fa);
  issue_w(rwo, 10, (2 + rot) & 15, S0);
  WAIT_V(4);
  rd_frag3(fb, S1, (1 + rot) & 15);
  mm(fa);
  pin(fb);
  issue_w(rwo, 10, (3 + rot) & 15, S1);
#pragma unroll 1
  for (int i = 2; i < 14; i += 2) {
    WAIT_V(4);
    rd_frag3(fa, S0, (i + rot) & 15);
    mm(fb);
    pin(fa);
    issue_w(rwo, 10, (i + 2 + rot) & 15, S0);
    WAIT_V(4);
    rd_frag3(fb, S1, (i + 1 + rot) & 15);
    mm(fa);
    pin(fb);
    issue_w(rwo, 10, (i + 3 + rot) & 15, S1);
  }
  WAIT_V(4);
  rd_frag3(fa, S0, (14 + rot) & 15);
  mm(fb);
  pin(fa);
  WAIT_V(0);
  rd_frag3(fb, S1, (15 + rot) & 15);
  mm(fa);
  pin(fb);
  mm(fb);

  STAMP(6);
  {
    char* part = p.ws + WS_PART;
    const __amdgpu_buffer_rsrc_t rs = __builtin_amdgcn_make_buffer_rsrc(part + (size_t)grp * 8 * 8 * 6144, 0, 8 * 8 * 6144, 0x00020000);
    const unsigned base = (unsigned)((w * 8 + kvh) * 6144);
#pragma unroll
    for (int c = 0; c < 4; ++c)
#pragma unroll
      for (int rt = 0; rt < 3; ++rt) {
        const unsigned off = base + (unsigned)((16 * rt + l15c) * 128 + (16 * c + 4 * gqc) * 2);
        u32x2 pk = {cvtpk(acc[c][rt][0], acc[c][rt][1]), cvtpk(acc[c][rt][2], acc[c][rt][3])};
        __builtin_amdgcn_raw_buffer_store_b64(pk, rs, off, 0, 16);
      }
    WAIT_V(0);
    BAR();
    STAMP(7);
    AS1 unsigned* flags = (AS1 unsigned*)(p.ws + WS_FLAG + (size_t)grp * 8 * 128);
    if (tid == 0) __hip_atomic_store(flags + kvh * 32, epoch, __ATOMIC_RELAXED, __HIP_MEMORY_SCOPE_AGENT);
    if (w == 0) {
      for (unsigned spins = 0; spins < 4000000u; ++spins) {
        unsigned f = epoch;
        if (lane < 8) f = __hip_atomic_load(flags + lane * 32, __ATOMIC_RELAXED, __HIP_MEMORY_SCOPE_AGENT);
        if (__all(f == epoch)) break;
        __builtin_amdgcn_s_sleep(1);
      }
      __builtin_amdgcn_fence(__ATOMIC_ACQUIRE, "agent");
      WAIT_V(0);
    }
    BAR();
    STAMP(8);
    if (tid == 0) __hip_atomic_store((AS1 unsigned*)(p.ws + WS_EPOCH + grp * 128), epoch, __ATOMIC_RELAXED, __HIP_MEMORY_SCOPE_AGENT);
    const char* src = part + (size_t)grp * 8 * 8 * 6144 + (size_t)kvh * 8 * 6144;
    char* outp = p.out + (size_t)b * 48 * 2048 + br * 1024 + kvh * 128;
    if (tid < 384) {
      u32x4 v[8];
#pragma unroll
      for (int sidx = 0; sidx < 8; ++sidx) v[sidx] = *(const u32x4*)(src + sidx * 6144 + tid * 16);
      float s[8];
#pragma unroll
      for (int j = 0; j < 8; ++j) s[j] = 0.f;
#pragma unroll
      for (int sidx = 0; sidx < 8; ++sidx)
#pragma unroll
        for (int j = 0; j < 4; ++j) { s[2 * j] += __uint_as_float(v[sidx][j] << 16); s[2 * j + 1] += __uint_as_float(v[sidx][j] & 0xffff0000u); }
      const int row = tid >> 3, c8 = tid & 7;
      u32x4 pk = {cvtpk(s[0], s[1]), cvtpk(s[2], s[3]), cvtpk(s[4], s[5]), cvtpk(s[6], s[7])};
      *(u32x4*)(outp + (size_t)row * 2048 + c8 * 16) = pk;
    }
  }
  STAMP(9);
}

extern "C" void kernel_launch(void* const* d_in, const int* in_sizes, int n_in, void* d_out, int out_size, void* d_ws, size_t ws_size,
                              hipStream_t stream) {
  Params p;
  for (int br = 0; br < 2; ++br) {
    p.x[br] = (const char*)d_in[5 * br + 0];
    p.k[br] = (const char*)d_in[5 * br + 1];
    p.v[br] = (const char*)d_in[5 * br + 2];
    p.wq[br] = (const char*)d_in[5 * br + 3];
    p.wo[br] = (const char*)d_in[5 * br + 4];
  }
  p.out = (char*)d_out;
  p.ws = (char*)d_ws;
  (void)hipFuncSetAttribute((const void*)fused_gqa_kernel, hipFuncAttributeMaxDynamicSharedMemorySize, LDS_BYTES);
  fused_gqa_kernel<<<256, 512, LDS_BYTES, stream>>>(p);
}
```

```cpp
#include <hip/hip_runtime.h>
#include <hip/hip_bf16.h>
#include <stdint.h>
#include <type_traits>

typedef short s16x4 __attribute__((ext_vector_type(4)));
typedef short s16x8 __attribute__((ext_vector_type(8)));
typedef __bf16 bf16x8 __attribute__((ext_vector_type(8)));
typedef __bf16 bf16x2 __attribute__((ext_vector_type(2)));
typedef float f32x4 __attribute__((ext_vector_type(4)));
typedef float f32x2 __attribute__((ext_vector_type(2)));
typedef unsigned u32x4 __attribute__((ext_vector_type(4)));
typedef unsigned u32x2 __attribute__((ext_vector_type(2)));

#define AS1 __attribute__((address_space(1)))
#define AS3 __attribute__((address_space(3)))
#define DI __device__ __forceinline__

struct Params {
  const char* x[2];
  const char* k[2];
  const char* v[2];
  const char* wq[2];
  const char* wo[2];
  char* out;
  char* ws;
};

constexpr unsigned XQ = 0;
constexpr unsigned RG = 49152;
constexpr unsigned UNIT = 16384;
constexpr int LDS_BYTES = 163840;

constexpr size_t WS_EPOCH = 0;
constexpr size_t WS_FLAG = 8192;
constexpr size_t WS_PART = 65536;

constexpr float CEXP = 0.08838834764831845f * 1.4426950408889634f;
constexpr float THR = 8.0f;

#define WAIT_V(n) asm volatile("s_waitcnt vmcnt(" #n ")" ::: "memory")
#define BAR() do { asm volatile("s_waitcnt lgkmcnt(0)" ::: "memory"); __builtin_amdgcn_s_barrier(); asm volatile("" ::: "memory"); } while (0)
#define STAMP(i) do {} while (0)

DI f32x4 mfma16(s16x8 a, s16x8 b, f32x4 c) {
  return __builtin_amdgcn_mfma_f32_16x16x32_bf16(__builtin_bit_cast(bf16x8, a), __builtin_bit_cast(bf16x8, b), c, 0, 0, 0);
}
DI unsigned cvtpk(float lo, float hi) {
  f32x2 v = {lo, hi};
  bf16x2 b = __builtin_convertvector(v, bf16x2);
  return __builtin_bit_cast(unsigned, b);
}
DI s16x4 trread(const char* lds, unsigned off) {
  return __builtin_amdgcn_ds_read_tr16_b64_v4i16((AS3 s16x4*)(lds + off));
}
DI s16x4 trreadA(unsigned a) { return __builtin_amdgcn_ds_read_tr16_b64_v4i16((AS3 s16x4*)(size_t)a); }
DI s16x8 trread2A(unsigned a0, unsigned a1) { s16x4 a = trreadA(a0), b = trreadA(a1); return __builtin_shufflevector(a, b, 0, 1, 2, 3, 4, 5, 6, 7); }
DI s16x8 ldsr128A(unsigned a) { return *(const AS3 s16x8*)(size_t)a; }
DI s16x8 trread2(const char* lds, unsigned off0, unsigned off1) {
  s16x4 a = trread(lds, off0), b = trread(lds, off1);
  return __builtin_shufflevector(a, b, 0, 1, 2, 3, 4, 5, 6, 7);
}
DI s16x8 ldsr128(const char* lds, unsigned off) { return *(const s16x8*)(lds + off); }
DI void dma_d(__amdgpu_buffer_rsrc_t rs, unsigned voff, unsigned soff, unsigned ldsaddr) {
  asm volatile("s_mov_b32 m0, %0\n\ts_nop 0\n\tbuffer_load_dwordx4 %1, %2, %3 offen lds" ::"s"(ldsaddr), "v"(voff), "s"(rs), "s"(soff) : "memory");
}
DI void dma_nt(__amdgpu_buffer_rsrc_t rs, unsigned voff, unsigned soff, unsigned ldsaddr) {
  asm volatile("s_mov_b32 m0, %0\n\ts_nop 0\n\tbuffer_load_dwordx4 %1, %2, %3 offen nt lds" ::"s"(ldsaddr), "v"(voff), "s"(rs), "s"(soff) : "memory");
}
DI __amdgpu_buffer_rsrc_t mkrsrc(const void* p, unsigned bytes) {
  return __builtin_amdgcn_make_buffer_rsrc((void*)p, 0, bytes, 0x00020000);
}
DI float xmax16_32(float v) {
  unsigned u = __float_as_uint(v);
  auto r = __builtin_amdgcn_permlane16_swap(u, u, false, false);
  v = fmaxf(__uint_as_float(r[0]), __uint_as_float(r[1]));
  u = __float_as_uint(v);
  auto r2 = __builtin_amdgcn_permlane32_swap(u, u, false, false);
  return fmaxf(__uint_as_float(r2[0]), __uint_as_float(r2[1]));
}
DI float xsum16_32(float v) {
  unsigned u = __float_as_uint(v);
  auto r = __builtin_amdgcn_permlane16_swap(u, u, false, false);
  v = __uint_as_float(r[0]) + __uint_as_float(r[1]);
  u = __float_as_uint(v);
  auto r2 = __builtin_amdgcn_permlane32_swap(u, u, false, false);
  return __uint_as_float(r2[0]) + __uint_as_float(r2[1]);
}

__global__ __launch_bounds__(512, 2) void fused_gqa_kernel(Params p) {
  extern __shared__ __attribute__((aligned(16))) char lds[];
  const int tid = threadIdx.x, lane = tid & 63;
  const int w = __builtin_amdgcn_readfirstlane(tid >> 6);
  const int bid = blockIdx.x;
  const int kvh = bid & 7, b = (bid >> 3) & 15, br = bid >> 7;
  const int grp = bid >> 3;
  const int l15 = lane & 15, gq = lane >> 4;
  const int tq = (lane & 15) >> 2, tp = lane & 3;
  const int g = w >> 1, hh = w & 1;
  const unsigned ldsb = (unsigned)(uintptr_t)lds;

  STAMP(0);
  const __amdgpu_buffer_rsrc_t rx = mkrsrc(p.x[br] + (size_t)b * 48 * 1024, 48 * 1024);
  const __amdgpu_buffer_rsrc_t rk = mkrsrc(p.k[br] + (size_t)(b * 8 + kvh) * 131072, 131072);
  const __amdgpu_buffer_rsrc_t rv = mkrsrc(p.v[br] + (size_t)(b * 8 + kvh) * 131072, 131072);
  const __amdgpu_buffer_rsrc_t rwq = mkrsrc(p.wq[br] + kvh * 1024, 512 * 8192 - kvh * 1024);
  const __amdgpu_buffer_rsrc_t rwo = mkrsrc(p.wo[br] + (size_t)kvh * 512 * 1024, 512 * 1024);

  const int wr = lane >> 3, wpos = lane & 7;
  const unsigned wsw0 = (unsigned)(wpos ^ (((wr >> 1) & 1) << 1)) * 16 + w * 128;

  unsigned wv0 = ((unsigned)wr << 13) + wsw0;
  auto issue_w = [&](__amdgpu_buffer_rsrc_t rs, int lgstride, int ks, unsigned stage_off) {
    const unsigned v0 = wv0, v1 = wv0 ^ 64;
#pragma unroll
    for (int i = 0; i < 4; ++i)
      dma_d(rs, (i & 1) ? v1 : v0, (unsigned)(ks * 32 + i * 8) << lgstride, ldsb + stage_off + w * 4096 + i * 1024);
  };
  const int kr = lane >> 4, kpos = lane & 15;
  unsigned kvo[2], vvo[2];
#pragma unroll
  for (int i = 0; i < 2; ++i) {
    const int row = (2 * w + i) * 4 + kr;
    kvo[i] = (unsigned)(row * 256 + ((kpos ^ (row & 15)) * 16));
    vvo[i] = (unsigned)(row * 256 + ((kpos ^ ((row & 7) << 1)) * 16));
  }
  auto issue_kv = [&](int n) {
    const unsigned dst = ldsb + RG + (unsigned)((4 + n) % 7) * UNIT + 2 * w * 1024;
    const unsigned so = (unsigned)(n >> 1) * 16384;
    if (n & 1) { dma_nt(rv, vvo[0], so, dst); dma_nt(rv, vvo[1], so, dst + 1024); }
    else       { dma_nt(rk, kvo[0], so, dst); dma_nt(rk, kvo[1], so, dst + 1024); }
  };

  const int rot = b;
#pragma unroll
  for (int i = 0; i < 6; ++i) {
    const int row = w * 6 + i;
    dma_d(rx, (unsigned)((lane ^ (row & 15)) * 16), (unsigned)row * 1024, ldsb + XQ + row * 1024);
  }
  issue_w(rwq, 13, (0 + rot) & 15, RG);
  issue_w(rwq, 13, (1 + rot) & 15, RG + 2 * UNIT);
  STAMP(12);
  __builtin_amdgcn_s_barrier();
  issue_kv(0); issue_kv(1); issue_kv(2);
  const unsigned epoch = __hip_atomic_load((const AS1 unsigned*)(p.ws + WS_EPOCH + grp * 128), __ATOMIC_RELAXED, __HIP_MEMORY_SCOPE_AGENT) + 1u;

  unsigned wtr[4];
  auto calc_wtr = [&](int ln) {
    const int gq_ = ln >> 4, tq_ = (ln & 15) >> 2, tp_ = ln & 3;
    const int wf = (((tq_ >> 1) & 1) << 1) | ((gq_ & 1) << 2);
#pragma unroll
    for (int c = 0; c < 4; ++c) wtr[c] = (unsigned)((8 * gq_ + tq_) * 128 + (((2 * c + (tp_ >> 1)) ^ wf) * 16) + 8 * (tp_ & 1)) + w * 4096;
  };
  calc_wtr(lane);

  struct Frag { s16x8 w[4]; s16x8 x[3]; };
  auto rd_frag1 = [&](Frag& f, unsigned stage_off, int ks) {
#pragma unroll
    for (int c = 0; c < 4; ++c) f.w[c] = trread2(lds, stage_off + wtr[c], stage_off + wtr[c] + 512);
#pragma unroll
    for (int rt = 0; rt < 3; ++rt) f.x[rt] = ldsr128(lds, XQ + (unsigned)((16 * rt + l15) * 1024 + (((4 * ks + gq) ^ l15) * 16)));
  };
  auto pin = [&](Frag& f) {
    asm volatile("s_waitcnt lgkmcnt(0)" : "+v"(f.w[0]), "+v"(f.w[1]), "+v"(f.w[2]), "+v"(f.w[3]), "+v"(f.x[0]), "+v"(f.x[1]), "+v"(f.x[2])::"memory");
  };
  f32x4 acc[4][3];
  auto mm = [&](Frag& f) {
#pragma unroll
    for (int c = 0; c < 4; ++c)
#pragma unroll
      for (int rt = 0; rt < 3; ++rt) acc[c][rt] = mfma16(f.w[c], f.x[rt], acc[c][rt]);
  };

#pragma unroll
  for (int c = 0; c < 4; ++c)
#pragma unroll
    for (int rt = 0; rt < 3; ++rt) acc[c][rt] = f32x4{0.f, 0.f, 0.f, 0.f};
  constexpr unsigned S0 = RG, S1 = RG + 2 * UNIT;
  Frag fa, fb;
  WAIT_V(11); BAR();
  STAMP(1);
  rd_frag1(fa, S0, (0 + rot) & 15); pin(fa);
  issue_w(rwq, 13, (2 + rot) & 15, S0);
  WAIT_V(11);
  rd_frag1(fb, S1, (1 + rot) & 15);
  mm(fa);
  pin(fb);
  issue_w(rwq, 13, (3 + rot) & 15, S1);
#pragma unroll 1
  for (int i = 2; i < 14; i += 2) {
    WAIT_V(4);
    rd_frag1(fa, S0, (i + rot) & 15);
    mm(fb);
    pin(fa);
    issue_w(rwq, 13, (i + 2 + rot) & 15, S0);
    WAIT_V(4);
    rd_frag1(fb, S1, (i + 1 + rot) & 15);
    mm(fa);
    pin(fb);
    issue_w(rwq, 13, (i + 3 + rot) & 15, S1);
  }
  WAIT_V(4);
  rd_frag1(fa, S0, (14 + rot) & 15);
  mm(fb);
  pin(fa);
  WAIT_V(0);
  rd_frag1(fb, S1, (15 + rot) & 15);
  mm(fa);
  pin(fb);
  BAR();
  STAMP(2);
  issue_kv(3); issue_kv(4); issue_kv(5); issue_kv(6);
  mm(fb);
#pragma unroll
  for (int c = 0; c < 4; ++c)
#pragma unroll
    for (int rt = 0; rt < 3; ++rt) {
      const int row = 16 * rt + l15;
      const int chunk = 8 * hh + 2 * c + (gq >> 1);
      u32x2 v = {cvtpk(acc[c][rt][0] * CEXP, acc[c][rt][1] * CEXP), cvtpk(acc[c][rt][2] * CEXP, acc[c][rt][3] * CEXP)};
      *(u32x2*)(lds + XQ + g * 12288 + row * 256 + ((chunk ^ l15) * 16) + (gq & 1) * 8) = v;
    }
  WAIT_V(12);
  BAR();
  s16x8 qf[3][4];
#pragma unroll
  for (int rt = 0; rt < 3; ++rt)
#pragma unroll
    for (int s = 0; s < 4; ++s) qf[rt][s] = ldsr128(lds, XQ + g * 12288 + (16 * rt + l15) * 256 + (((4 * s + gq) ^ l15) * 16));

  wv0 = (wv0 & 8191u) + ((wv0 >> 13) << 10);
  STAMP(3);
  f32x4 o[8][3];
#pragma unroll
  for (int c = 0; c < 8; ++c)
#pragma unroll
    for (int rt = 0; rt < 3; ++rt) o[c][rt] = f32x4{0.f, 0.f, 0.f, 0.f};
  f32x4 lacc[3];
#pragma unroll
  for (int rt = 0; rt < 3; ++rt) lacc[rt] = f32x4{0.f, 0.f, 0.f, 0.f};
  const s16x8 ones = {0x3F80, 0x3F80, 0x3F80, 0x3F80, 0x3F80, 0x3F80, 0x3F80, 0x3F80};
  const unsigned koff = (unsigned)((32 * hh + l15) * 256 + ((gq ^ l15) * 16));
  const int vrow = 32 * hh + 4 * gq + tq;
  const int vsw = (vrow & 7) << 1;
  const unsigned voff = (unsigned)(vrow * 256 + (((tp >> 1) ^ vsw) * 16) + 8 * (tp & 1));

  typedef f32x4 Stile[2][3];
#define SB() __builtin_amdgcn_sched_barrier(0)
  auto max3a = [](float a, float b2, float c) { float r; asm("v_max3_f32 %0, %1, %2, %3" : "=v"(r) : "v"(a), "v"(b2), "v"(c)); return r; };
  auto max2a = [](float a, float b2) { float r; asm("v_max_f32_e32 %0, %1, %2" : "=v"(r) : "v"(a), "v"(b2)); return r; };
  auto xmax = [&](float v) {
    unsigned u = __float_as_uint(v);
    auto r = __builtin_amdgcn_permlane16_swap(u, u, false, false);
    v = max2a(__uint_as_float(r[0]), __uint_as_float(r[1]));
    u = __float_as_uint(v);
    auto r2 = __builtin_amdgcn_permlane32_swap(u, u, false, false);
    return max2a(__uint_as_float(r2[0]), __uint_as_float(r2[1]));
  };
  auto uoff = [&](int n) { return RG + (unsigned)((4 + n) % 7) * UNIT; };
  auto step_top = [&](int t) {
    if (t == 0) WAIT_V(8);
    else if (t <= 5) WAIT_V(6);
    else if (t == 6) WAIT_V(2);
    else WAIT_V(4);
    BAR();
    if (t == 0) issue_kv(7);
    else if (t <= 4) { issue_kv(2 * t + 6); issue_kv(2 * t + 7); }
    else if (t == 6) issue_w(rwo, 10, (0 + rot) & 15, S0);
    else if (t == 7) issue_w(rwo, 10, (1 + rot) & 15, S1);
  };
  s16x8 pf[3];
  f32x4 negm[3];
  auto stepAB = [&](Stile& sn, Stile& sc, unsigned ku, unsigned vu, float& lmax, auto DOQK, auto DOSM, auto CINIT) {
    constexpr bool doqk = decltype(DOQK)::value, dosm = decltype(DOSM)::value, cinit = decltype(CINIT)::value;
    s16x8 kf[8], vf[8];
    unsigned kb = ldsb + ku + koff; asm volatile("" : "+v"(kb));
    unsigned vb = ldsb + vu + voff; asm volatile("" : "+v"(vb));
    float e[24];
    if constexpr (doqk) {
      kf[0] = ldsr128A(kb);
      kf[1] = ldsr128A(kb + 4096);
      kf[2] = ldsr128A(kb ^ 64u);
    }
#pragma unroll
    for (int j = 0; j < 8; ++j) {
      if constexpr (doqk) {
        if (j + 3 < 8) kf[j + 3] = ldsr128A((kb ^ (unsigned)(((j + 3) >> 1) << 6)) + ((j + 3) & 1) * 4096);
      }
      if constexpr (dosm) {
        if (j >= 6) { const unsigned a = vb ^ (unsigned)((j - 6) << 5); vf[j - 6] = trread2A(a, a + 4096); }
#pragma unroll
        for (int u = 0; u < 3; ++u) {
          const int q = 3 * j + u, rt = q >> 3, idx = q & 7;
          e[q] = __builtin_amdgcn_exp2f(sc[idx >> 2][rt][idx & 3]);
        }
      }
      if constexpr (doqk) {
#pragma unroll
        for (int rt = 0; rt < 3; ++rt) {
          if (j < 2) sn[j & 1][rt] = mfma16(kf[j], qf[rt][j >> 1], cinit ? negm[rt] : f32x4{0.f, 0.f, 0.f, 0.f});
          else sn[j & 1][rt] = mfma16(kf[j], qf[rt][j >> 1], sn[j & 1][rt]);
        }
      }
      if constexpr (dosm) {
#pragma unroll
        for (int pr = 0; pr < 12; ++pr) {
          const int hi_el = 2 * pr + 1;
          if (hi_el / 3 == j) {
            const int rt = pr >> 2;
            unsigned pk = cvtpk(e[2 * pr], e[2 * pr + 1]);
            u32x4 t4 = __builtin_bit_cast(u32x4, pf[rt]); t4[pr & 3] = pk; pf[rt] = __builtin_bit_cast(s16x8, t4);
          }
        }
      }
      SB();
    }
    if constexpr (dosm) {
      { const unsigned a = vb ^ 64u; vf[2] = trread2A(a, a + 4096); }
      { const unsigned a = vb ^ 96u; vf[3] = trread2A(a, a + 4096); }
      asm volatile("s_nop 7" ::: "memory");
      SB();
#pragma unroll
      for (int rt = 0; rt < 3; ++rt) lacc[rt] = mfma16(ones, pf[rt], lacc[rt]);
      float t0 = 0.f;
#pragma unroll
      for (int c = 0; c < 8; ++c) {
        if (c + 4 < 8) { const unsigned a = vb ^ (unsigned)((c + 4) << 5); vf[c + 4] = trread2A(a, a + 4096); }
#pragma unroll
        for (int rt = 0; rt < 3; ++rt) o[c][rt] = mfma16(vf[c], pf[rt], o[c][rt]);
        if constexpr (doqk) {
          if (c < 6) {
            const int rt = c >> 1, kt = c & 1;
            if (c == 0) t0 = max3a(sn[0][0][0], sn[0][0][1], sn[0][0][2]);
            else t0 = max3a(t0, sn[kt][rt][0], sn[kt][rt][1]);
            if (c == 0) t0 = max2a(t0, sn[0][0][3]); else t0 = max3a(t0, sn[kt][rt][2], sn[kt][rt][3]);
          }
        }
        SB();
      }
      lmax = t0;
    }
  };
  using T_ = std::integral_constant<bool, true>; using F_ = std::integral_constant<bool, false>;
  auto decide = [&](Stile& sn, float lmax) {
    if (__builtin_expect(__any(lmax > THR), 0)) {
#pragma unroll
      for (int rt = 0; rt < 3; ++rt) {
        float a = fmaxf(fmaxf(sn[0][rt][0], sn[0][rt][1]), fmaxf(sn[0][rt][2], sn[0][rt][3]));
        float c2 = fmaxf(fmaxf(sn[1][rt][0], sn[1][rt][1]), fmaxf(sn[1][rt][2], sn[1][rt][3]));
        const float dl = fmaxf(xmax16_32(fmaxf(a, c2)), 0.f);
        const float al = __builtin_amdgcn_exp2f(-dl);
        negm[rt] -= dl; lacc[rt] *= al;
        sn[0][rt] -= dl; sn[1][rt] -= dl;
#pragma unroll
        for (int c = 0; c < 8; ++c) o[c][rt] *= al;
      }
    }
  };

  Stile sa, sb;
  float lmax;
  stepAB(sa, sb, uoff(0), 0, lmax, T_{}, F_{}, F_{});
#pragma unroll
  for (int rt = 0; rt < 3; ++rt) {
    float a = fmaxf(fmaxf(sa[0][rt][0], sa[0][rt][1]), fmaxf(sa[0][rt][2], sa[0][rt][3]));
    float c2 = fmaxf(fmaxf(sa[1][rt][0], sa[1][rt][1]), fmaxf(sa[1][rt][2], sa[1][rt][3]));
    const float m0 = xmax16_32(fmaxf(a, c2));
    negm[rt] = f32x4{-m0, -m0, -m0, -m0};
    sa[0][rt] -= m0; sa[1][rt] -= m0;
  }
#define CY(acc) do {} while (0)
#pragma unroll 1
  for (int t = 0; t < 6; t += 2) {
    step_top(t);
    CY(cyT);
    stepAB(sb, sa, uoff(2 * t + 2), uoff(2 * t + 1), lmax, T_{}, T_{}, T_{});
    decide(sb, lmax);
    CY(cyA);
    step_top(t + 1);
    CY(cyT);
    stepAB(sa, sb, uoff(2 * t + 4), uoff(2 * t + 3), lmax, T_{}, T_{}, T_{});
    decide(sa, lmax);
    CY(cyA);
  }
  step_top(6);
  stepAB(sb, sa, uoff(14), uoff(13), lmax, T_{}, T_{}, T_{});
  decide(sb, lmax);
  step_top(7);
  stepAB(sa, sb, 0, uoff(15), lmax, F_{}, T_{}, F_{});

  STAMP(4);
  float mrow[3], lrow[3];
#pragma unroll
  for (int rt = 0; rt < 3; ++rt) { lrow[rt] = lacc[rt][0]; mrow[rt] = -negm[rt][0]; }
  const unsigned eoff_me = (w < 4) ? XQ + w * 12288 : RG + 4 * UNIT + (w - 4) * 12288;
  const unsigned eoff_pt = ((w ^ 1) < 4) ? XQ + (w ^ 1) * 12288 : RG + 4 * UNIT + ((w ^ 1) - 4) * 12288;
  {
    float* stat = (float*)(lds + XQ);
    if (gq == 0) {
#pragma unroll
      for (int rt = 0; rt < 3; ++rt) { stat[((w * 3 + rt) * 16 + l15) * 2] = mrow[rt]; stat[((w * 3 + rt) * 16 + l15) * 2 + 1] = lrow[rt]; }
    }
    BAR();
    STAMP(13);
    float sc[3];
#pragma unroll
    for (int rt = 0; rt < 3; ++rt) {
      const float m2 = stat[(((w ^ 1) * 3 + rt) * 16 + l15) * 2], l2 = stat[(((w ^ 1) * 3 + rt) * 16 + l15) * 2 + 1];
      const float mn = fmaxf(mrow[rt], m2);
      const float w1 = __builtin_amdgcn_exp2f(mrow[rt] - mn), w2 = __builtin_amdgcn_exp2f(m2 - mn);
      sc[rt] = w1 / (lrow[rt] * w1 + l2 * w2);
    }
    BAR();
#pragma unroll
    for (int cc = 0; cc < 4; ++cc)
#pragma unroll
      for (int rt = 0; rt < 3; ++rt) {
        o[cc][rt] *= sc[rt]; o[4 + cc][rt] *= sc[rt];
        const f32x4 v = hh ? o[cc][rt] : o[4 + cc][rt];
        *(f32x4*)(lds + eoff_me + (cc * 3 + rt) * 1024 + lane * 16) = v;
      }
    BAR();
    STAMP(14);
#pragma unroll
    for (int cc = 0; cc < 4; ++cc)
#pragma unroll
      for (int rt = 0; rt < 3; ++rt) {
        const f32x4 r = *(const f32x4*)(lds + eoff_pt + (cc * 3 + rt) * 1024 + lane * 16);
        const f32x4 v = (hh ? o[4 + cc][rt] : o[cc][rt]) + r;
        o[cc][rt] = v;
      }
    asm volatile("s_waitcnt lgkmcnt(0)" ::: "memory");
#pragma unroll
    for (int cc = 0; cc < 4; ++cc)
#pragma unroll
      for (int rt = 0; rt < 3; ++rt) {
        const f32x4 v = o[cc][rt];
        const int row = 16 * rt + l15;
        const int chunk = 2 * cc + (gq >> 1);
        u32x2 pk = {cvtpk(v[0], v[1]), cvtpk(v[2], v[3])};
        *(u32x2*)(lds + eoff_pt + row * 128 + ((chunk ^ ((l15 >> 1) & 7)) * 16) + (gq & 1) * 8) = pk;
      }
    BAR();
  }

  STAMP(5);
  int lane3 = lane; asm volatile("" : "+v"(lane3));
  calc_wtr(lane3);
  const int l15c = lane3 & 15, gqc = lane3 >> 4;
  auto rd_frag3 = [&](Frag& f, unsigned stage_off, int ks) {
#pragma unroll
    for (int c = 0; c < 4; ++c) f.w[c] = trread2(lds, stage_off + wtr[c], stage_off + wtr[c] + 512);
    const int wb = (ks >> 1) ^ 1;
    const unsigned blk = (wb < 4) ? XQ + wb * 12288 : RG + 4 * UNIT + (wb - 4) * 12288;
#pragma unroll
    for (int rt = 0; rt < 3; ++rt) f.x[rt] = ldsr128(lds, blk + (unsigned)((16 * rt + l15c) * 128 + (((4 * (ks & 1) + gqc) ^ ((l15c >> 1) & 7)) * 16)));
  };
#pragma unroll
  for (int c = 0; c < 4; ++c)
#pragma unroll
    for (int rt = 0; rt < 3; ++rt) acc[c][rt] = f32x4{0.f, 0.f, 0.f, 0.f};
  WAIT_V(4);
  rd_frag3(fa, S0, (0 + rot) & 15); pin(fa);
  issue_w(rwo, 10, (2 + rot) & 15, S0);
  WAIT_V(4);
  rd_frag3(fb, S1, (1 + rot) & 15);
  mm(fa);
  pin(fb);
  issue_w(rwo, 10, (3 + rot) & 15, S1);
#pragma unroll 1
  for (int i = 2; i < 14; i += 2) {
    WAIT_V(4);
    rd_frag3(fa, S0, (i + rot) & 15);
    mm(fb);
    pin(fa);
    issue_w(rwo, 10, (i + 2 + rot) & 15, S0);
    WAIT_V(4);
    rd_frag3(fb, S1, (i + 1 + rot) & 15);
    mm(fa);
    pin(fb);
    issue_w(rwo, 10, (i + 3 + rot) & 15, S1);
  }
  WAIT_V(4);
  rd_frag3(fa, S0, (14 + rot) & 15);
  mm(fb);
  pin(fa);
  WAIT_V(0);
  rd_frag3(fb, S1, (15 + rot) & 15);
  mm(fa);
  pin(fb);
  mm(fb);

  STAMP(6);
  {
    char* part = p.ws + WS_PART;
    const __amdgpu_buffer_rsrc_t rs = __builtin_amdgcn_make_buffer_rsrc(part + (size_t)grp * 8 * 8 * 6144, 0, 8 * 8 * 6144, 0x00020000);
    const unsigned base = (unsigned)((w * 8 + kvh) * 6144);
#pragma unroll
    for (int cp = 0; cp < 2; ++cp)
#pragma unroll
      for (int rt = 0; rt < 3; ++rt) {
        const f32x4 a0 = acc[2 * cp][rt], a1 = acc[2 * cp + 1][rt];
        auto r0 = __builtin_amdgcn_permlane16_swap(cvtpk(a0[0], a0[1]), cvtpk(a1[0], a1[1]), false, false);
        auto r1 = __builtin_amdgcn_permlane16_swap(cvtpk(a0[2], a0[3]), cvtpk(a1[2], a1[3]), false, false);
        u32x4 pk = {r0[0], r1[0], r0[1], r1[1]};
        const unsigned off = base + (unsigned)((16 * rt + l15c) * 128 + (16 * (2 * cp + (gqc & 1)) + 8 * (gqc >> 1)) * 2);
        __builtin_amdgcn_raw_buffer_store_b128(pk, rs, off, 0, 16);
      }
    WAIT_V(0);
    BAR();
    STAMP(7);
    AS1 unsigned* flags = (AS1 unsigned*)(p.ws + WS_FLAG + (size_t)grp * 8 * 128);
    if (tid == 0) __hip_atomic_store(flags + kvh * 32, epoch, __ATOMIC_RELAXED, __HIP_MEMORY_SCOPE_AGENT);
    if (w == 0) {
      for (unsigned spins = 0; spins < 4000000u; ++spins) {
        unsigned f = epoch;
        if (lane < 8) f = __hip_atomic_load(flags + lane * 32, __ATOMIC_RELAXED, __HIP_MEMORY_SCOPE_AGENT);
        if (__all(f == epoch)) break;
        __builtin_amdgcn_s_sleep(1);
      }
      __builtin_amdgcn_fence(__ATOMIC_ACQUIRE, "agent");
      WAIT_V(0);
    }
    BAR();
    STAMP(8);
    if (tid == 0) __hip_atomic_store((AS1 unsigned*)(p.ws + WS_EPOCH + grp * 128), epoch, __ATOMIC_RELAXED, __HIP_MEMORY_SCOPE_AGENT);
    const char* src = part + (size_t)grp * 8 * 8 * 6144 + (size_t)kvh * 8 * 6144;
    char* outp = p.out + (size_t)b * 48 * 2048 + br * 1024 + kvh * 128;
    if (tid < 384) {
      u32x4 v[8];
#pragma unroll
      for (int sidx = 0; sidx < 8; ++sidx) v[sidx] = *(const u32x4*)(src + sidx * 6144 + tid * 16);
      float s[8];
#pragma unroll
      for (int j = 0; j < 8; ++j) s[j] = 0.f;
#pragma unroll
      for (int sidx = 0; sidx < 8; ++sidx)
#pragma unroll
        for (int j = 0; j < 4; ++j) { s[2 * j] += __uint_as_float(v[sidx][j] << 16); s[2 * j + 1] += __uint_as_float(v[sidx][j] & 0xffff0000u); }
      const int row = tid >> 3, c8 = tid & 7;
      u32x4 pk = {cvtpk(s[0], s[1]), cvtpk(s[2], s[3]), cvtpk(s[4], s[5]), cvtpk(s[6], s[7])};
      *(u32x4*)(outp + (size_t)row * 2048 + c8 * 16) = pk;
    }
  }
  STAMP(9);
}

extern "C" void kernel_launch(void* const* d_in, const int* in_sizes, int n_in, void* d_out, int out_size, void* d_ws, size_t ws_size,
                              hipStream_t stream) {
  Params p;
  for (int br = 0; br < 2; ++br) {
    p.x[br] = (const char*)d_in[5 * br + 0];
    p.k[br] = (const char*)d_in[5 * br + 1];
    p.v[br] = (const char*)d_in[5 * br + 2];
    p.wq[br] = (const char*)d_in[5 * br + 3];
    p.wo[br] = (const char*)d_in[5 * br + 4];
  }
  p.out = (char*)d_out;
  p.ws = (char*)d_ws;
  (void)hipFuncSetAttribute((const void*)fused_gqa_kernel, hipFuncAttributeMaxDynamicSharedMemorySize, LDS_BYTES);
  fused_gqa_kernel<<<256, 512, LDS_BYTES, stream>>>(p);
}
```

```cpp
#include <hip/hip_runtime.h>
#include <hip/hip_bf16.h>
#include <stdint.h>
#include <type_traits>

typedef short s16x4 __attribute__((ext_vector_type(4)));
typedef short s16x8 __attribute__((ext_vector_type(8)));
typedef __bf16 bf16x8 __attribute__((ext_vector_type(8)));
typedef __bf16 bf16x2 __attribute__((ext_vector_type(2)));
typedef float f32x4 __attribute__((ext_vector_type(4)));
typedef float f32x2 __attribute__((ext_vector_type(2)));
typedef unsigned u32x4 __attribute__((ext_vector_type(4)));
typedef unsigned u32x2 __attribute__((ext_vector_type(2)));

#define AS1 __attribute__((address_space(1)))
#define AS3 __attribute__((address_space(3)))
#define DI __device__ __forceinline__

struct Params {
  const char* x[2];
  const char* k[2];
  const char* v[2];
  const char* wq[2];
  const char* wo[2];
  char* out;
  char* ws;
};

constexpr unsigned XQ = 0;
constexpr unsigned RG = 49152;
constexpr unsigned UNIT = 16384;
constexpr int LDS_BYTES = 163840;

constexpr size_t WS_EPOCH = 0;
constexpr size_t WS_FLAG = 8192;
constexpr size_t WS_PART = 65536;

constexpr float CEXP = 0.08838834764831845f * 1.4426950408889634f;
constexpr float THR = 8.0f;

#define WAIT_V(n) asm volatile("s_waitcnt vmcnt(" #n ")" ::: "memory")
#define BAR() do { asm volatile("s_waitcnt lgkmcnt(0)" ::: "memory"); __builtin_amdgcn_s_barrier(); asm volatile("" ::: "memory"); } while (0)
#define STAMP(i) do {} while (0)

DI f32x4 mfma16(s16x8 a, s16x8 b, f32x4 c) {
  return __builtin_amdgcn_mfma_f32_16x16x32_bf16(__builtin_bit_cast(bf16x8, a), __builtin_bit_cast(bf16x8, b), c, 0, 0, 0);
}
DI unsigned cvtpk(float lo, float hi) {
  f32x2 v = {lo, hi};
  bf16x2 b = __builtin_convertvector(v, bf16x2);
  return __builtin_bit_cast(unsigned, b);
}
DI s16x4 trread(const char* lds, unsigned off) {
  return __builtin_amdgcn_ds_read_tr16_b64_v4i16((AS3 s16x4*)(lds + off));
}
DI s16x4 trreadA(unsigned a) { return __builtin_amdgcn_ds_read_tr16_b64_v4i16((AS3 s16x4*)(size_t)a); }
DI s16x8 trread2A(unsigned a0, unsigned a1) { s16x4 a = trreadA(a0), b = trreadA(a1); return __builtin_shufflevector(a, b, 0, 1, 2, 3, 4, 5, 6, 7); }
DI s16x8 ldsr128A(unsigned a) { return *(const AS3 s16x8*)(size_t)a; }
DI s16x8 trread2(const char* lds, unsigned off0, unsigned off1) {
  s16x4 a = trread(lds, off0), b = trread(lds, off1);
  return __builtin_shufflevector(a, b, 0, 1, 2, 3, 4, 5, 6, 7);
}
DI s16x8 ldsr128(const char* lds, unsigned off) { return *(const s16x8*)(lds + off); }
DI void dma_d(__amdgpu_buffer_rsrc_t rs, unsigned voff, unsigned soff, unsigned ldsaddr) {
  asm volatile("s_mov_b32 m0, %0\n\ts_nop 0\n\tbuffer_load_dwordx4 %1, %2, %3 offen lds" ::"s"(ldsaddr), "v"(voff), "s"(rs), "s"(soff) : "memory");
}
DI void dma_nt(__amdgpu_buffer_rsrc_t rs, unsigned voff, unsigned soff, unsigned ldsaddr) {
  asm volatile("s_mov_b32 m0, %0\n\ts_nop 0\n\tbuffer_load_dwordx4 %1, %2, %3 offen nt lds" ::"s"(ldsaddr), "v"(voff), "s"(rs), "s"(soff) : "memory");
}
DI __amdgpu_buffer_rsrc_t mkrsrc(const void* p, unsigned bytes) {
  return __builtin_amdgcn_make_buffer_rsrc((void*)p, 0, bytes, 0x00020000);
}
DI float xmax16_32(float v) {
  unsigned u = __float_as_uint(v);
  auto r = __builtin_amdgcn_permlane16_swap(u, u, false, false);
  v = fmaxf(__uint_as_float(r[0]), __uint_as_float(r[1]));
  u = __float_as_uint(v);
  auto r2 = __builtin_amdgcn_permlane32_swap(u, u, false, false);
  return fmaxf(__uint_as_float(r2[0]), __uint_as_float(r2[1]));
}
DI float xsum16_32(float v) {
  unsigned u = __float_as_uint(v);
  auto r = __builtin_amdgcn_permlane16_swap(u, u, false, false);
  v = __uint_as_float(r[0]) + __uint_as_float(r[1]);
  u = __float_as_uint(v);
  auto r2 = __builtin_amdgcn_permlane32_swap(u, u, false, false);
  return __uint_as_float(r2[0]) + __uint_as_float(r2[1]);
}

__global__ __launch_bounds__(512, 2) void fused_gqa_kernel(Params p) {
  extern __shared__ __attribute__((aligned(16))) char lds[];
  const int tid = threadIdx.x, lane = tid & 63;
  const int w = __builtin_amdgcn_readfirstlane(tid >> 6);
  const int bid = blockIdx.x;
  const int kvh = bid & 7, b = (bid >> 3) & 15, br = bid >> 7;
  const int grp = bid >> 3;
  const int l15 = lane & 15, gq = lane >> 4;
  const int tq = (lane & 15) >> 2, tp = lane & 3;
  const int g = w >> 1, hh = w & 1;
  const unsigned ldsb = (unsigned)(uintptr_t)lds;

  STAMP(0);
  const __amdgpu_buffer_rsrc_t rx = mkrsrc(p.x[br] + (size_t)b * 48 * 1024, 48 * 1024);
  const __amdgpu_buffer_rsrc_t rk = mkrsrc(p.k[br] + (size_t)(b * 8 + kvh) * 131072, 131072);
  const __amdgpu_buffer_rsrc_t rv = mkrsrc(p.v[br] + (size_t)(b * 8 + kvh) * 131072, 131072);
  const __amdgpu_buffer_rsrc_t rwq = mkrsrc(p.wq[br] + kvh * 1024, 512 * 8192 - kvh * 1024);
  const __amdgpu_buffer_rsrc_t rwo = mkrsrc(p.wo[br] + (size_t)kvh * 512 * 1024, 512 * 1024);

  const int wr = lane >> 3, wpos = lane & 7;
  const unsigned wsw0 = (unsigned)(wpos ^ (((wr >> 1) & 1) << 1)) * 16 + w * 128;

  unsigned wv0 = ((unsigned)wr << 13) + wsw0;
  auto issue_w = [&](__amdgpu_buffer_rsrc_t rs, int lgstride, int ks, unsigned stage_off) {
    const unsigned v0 = wv0, v1 = wv0 ^ 64;
#pragma unroll
    for (int i = 0; i < 4; ++i)
      dma_d(rs, (i & 1) ? v1 : v0, (unsigned)(ks * 32 + i * 8) << lgstride, ldsb + stage_off + w * 4096 + i * 1024);
  };
  const int kr = lane >> 4, kpos = lane & 15;
  unsigned kvo[2], vvo[2];
#pragma unroll
  for (int i = 0; i < 2; ++i) {
    const int row = (2 * w + i) * 4 + kr;
    kvo[i] = (unsigned)(row * 256 + ((kpos ^ (row & 15)) * 16));
    vvo[i] = (unsigned)(row * 256 + ((kpos ^ ((row & 7) << 1)) * 16));
  }
  auto issue_kv = [&](int n) {
    const unsigned dst = ldsb + RG + (unsigned)((4 + n) % 7) * UNIT + 2 * w * 1024;
    const unsigned so = (unsigned)(n >> 1) * 16384;
    if (n & 1) { dma_nt(rv, vvo[0], so, dst); dma_nt(rv, vvo[1], so, dst + 1024); }
    else       { dma_nt(rk, kvo[0], so, dst); dma_nt(rk, kvo[1], so, dst + 1024); }
  };

  const int rot = b;
#pragma unroll
  for (int i = 0; i < 6; ++i) {
    const int row = w * 6 + i;
    dma_d(rx, (unsigned)((lane ^ (row & 15)) * 16), (unsigned)row * 1024, ldsb + XQ + row * 1024);
  }
  issue_w(rwq, 13, (0 + rot) & 15, RG);
  issue_w(rwq, 13, (1 + rot) & 15, RG + 2 * UNIT);
  STAMP(12);
  __builtin_amdgcn_s_barrier();
  issue_kv(0); issue_kv(1); issue_kv(2);
  const unsigned epoch = __hip_atomic_load((const AS1 unsigned*)(p.ws + WS_EPOCH + grp * 128), __ATOMIC_RELAXED, __HIP_MEMORY_SCOPE_AGENT) + 1u;

  unsigned wtr[4];
  auto calc_wtr = [&](int ln) {
    const int gq_ = ln >> 4, tq_ = (ln & 15) >> 2, tp_ = ln & 3;
    const int wf = (((tq_ >> 1) & 1) << 1) | ((gq_ & 1) << 2);
#pragma unroll
    for (int c = 0; c < 4; ++c) wtr[c] = (unsigned)((8 * gq_ + tq_) * 128 + (((2 * c + (tp_ >> 1)) ^ wf) * 16) + 8 * (tp_ & 1)) + w * 4096;
  };
  calc_wtr(lane);

  struct Frag { s16x8 w[4]; s16x8 x[3]; };
  auto rd_frag1 = [&](Frag& f, unsigned stage_off, int ks) {
#pragma unroll
    for (int c = 0; c < 4; ++c) f.w[c] = trread2(lds, stage_off + wtr[c], stage_off + wtr[c] + 512);
#pragma unroll
    for (int rt = 0; rt < 3; ++rt) f.x[rt] = ldsr128(lds, XQ + (unsigned)((16 * rt + l15) * 1024 + (((4 * ks + gq) ^ l15) * 16)));
  };
  auto pin = [&](Frag& f) {
    asm volatile("s_waitcnt lgkmcnt(0)" : "+v"(f.w[0]), "+v"(f.w[1]), "+v"(f.w[2]), "+v"(f.w[3]), "+v"(f.x[0]), "+v"(f.x[1]), "+v"(f.x[2])::"memory");
  };
  f32x4 acc[4][3];
  auto mm = [&](Frag& f) {
#pragma unroll
    for (int c = 0; c < 4; ++c)
#pragma unroll
      for (int rt = 0; rt < 3; ++rt) acc[c][rt] = mfma16(f.w[c], f.x[rt], acc[c][rt]);
  };

#pragma unroll
  for (int c = 0; c < 4; ++c)
#pragma unroll
    for (int rt = 0; rt < 3; ++rt) acc[c][rt] = f32x4{0.f, 0.f, 0.f, 0.f};
  constexpr unsigned S0 = RG, S1 = RG + 2 * UNIT;
  Frag fa, fb;
  WAIT_V(11); BAR();
  STAMP(1);
  rd_frag1(fa, S0, (0 + rot) & 15); pin(fa);
  issue_w(rwq, 13, (2 + rot) & 15, S0);
  WAIT_V(11);
  rd_frag1(fb, S1, (1 + rot) & 15);
  mm(fa);
  pin(fb);
  issue_w(rwq, 13, (3 + rot) & 15, S1);
#define CY1(acc) do {} while (0)
#pragma unroll 1
  for (int i = 2; i < 14; i += 2) {
    WAIT_V(4);
    CY1(cW);
    rd_frag1(fa, S0, (i + rot) & 15);
    mm(fb);
    pin(fa);
    CY1(cC);
    issue_w(rwq, 13, (i + 2 + rot) & 15, S0);
    CY1(cI);
    WAIT_V(4);
    CY1(cW);
    rd_frag1(fb, S1, (i + 1 + rot) & 15);
    mm(fa);
    pin(fb);
    CY1(cC);
    issue_w(rwq, 13, (i + 3 + rot) & 15, S1);
    CY1(cI);
  }
  WAIT_V(4);
  rd_frag1(fa, S0, (14 + rot) & 15);
  mm(fb);
  pin(fa);
  WAIT_V(0);
  rd_frag1(fb, S1, (15 + rot) & 15);
  mm(fa);
  pin(fb);
  mm(fb);
  BAR();
  STAMP(2);
#pragma unroll
  for (int c = 0; c < 4; ++c)
#pragma unroll
    for (int rt = 0; rt < 3; ++rt) {
      const int row = 16 * rt + l15;
      const int chunk = 8 * hh + 2 * c + (gq >> 1);
      u32x2 v = {cvtpk(acc[c][rt][0] * CEXP, acc[c][rt][1] * CEXP), cvtpk(acc[c][rt][2] * CEXP, acc[c][rt][3] * CEXP)};
      *(u32x2*)(lds + XQ + g * 12288 + row * 256 + ((chunk ^ l15) * 16) + (gq & 1) * 8) = v;
    }
  BAR();
  s16x8 qf[3][4];
#pragma unroll
  for (int rt = 0; rt < 3; ++rt)
#pragma unroll
    for (int s = 0; s < 4; ++s) qf[rt][s] = ldsr128(lds, XQ + g * 12288 + (16 * rt + l15) * 256 + (((4 * s + gq) ^ l15) * 16));
  issue_kv(3); issue_kv(4);

  wv0 = (wv0 & 8191u) + ((wv0 >> 13) << 10);
  STAMP(3);
  f32x4 o[8][3];
#pragma unroll
  for (int c = 0; c < 8; ++c)
#pragma unroll
    for (int rt = 0; rt < 3; ++rt) o[c][rt] = f32x4{0.f, 0.f, 0.f, 0.f};
  f32x4 lacc[3];
#pragma unroll
  for (int rt = 0; rt < 3; ++rt) lacc[rt] = f32x4{0.f, 0.f, 0.f, 0.f};
  const s16x8 ones = {0x3F80, 0x3F80, 0x3F80, 0x3F80, 0x3F80, 0x3F80, 0x3F80, 0x3F80};
  const unsigned koff = (unsigned)((32 * hh + l15) * 256 + ((gq ^ l15) * 16));
  const int vrow = 32 * hh + 4 * gq + tq;
  const int vsw = (vrow & 7) << 1;
  const unsigned voff = (unsigned)(vrow * 256 + (((tp >> 1) ^ vsw) * 16) + 8 * (tp & 1));

  typedef f32x4 Stile[2][3];
#define SB() __builtin_amdgcn_sched_barrier(0)
  auto max3a = [](float a, float b2, float c) { float r; asm("v_max3_f32 %0, %1, %2, %3" : "=v"(r) : "v"(a), "v"(b2), "v"(c)); return r; };
  auto max2a = [](float a, float b2) { float r; asm("v_max_f32_e32 %0, %1, %2" : "=v"(r) : "v"(a), "v"(b2)); return r; };
  auto xmax = [&](float v) {
    unsigned u = __float_as_uint(v);
    auto r = __builtin_amdgcn_permlane16_swap(u, u, false, false);
    v = max2a(__uint_as_float(r[0]), __uint_as_float(r[1]));
    u = __float_as_uint(v);
    auto r2 = __builtin_amdgcn_permlane32_swap(u, u, false, false);
    return max2a(__uint_as_float(r2[0]), __uint_as_float(r2[1]));
  };
  auto uoff = [&](int n) { return RG + (unsigned)((4 + n) % 7) * UNIT; };
  auto step_top = [&](int t) {
    if (t == 0) WAIT_V(8);
    else if (t <= 5) WAIT_V(6);
    else if (t == 6) WAIT_V(2);
    else WAIT_V(4);
    BAR();
    if (t == 0) issue_kv(7);
    else if (t <= 4) { issue_kv(2 * t + 6); issue_kv(2 * t + 7); }
    else if (t == 6) issue_w(rwo, 10, (0 + rot) & 15, S0);
    else if (t == 7) issue_w(rwo, 10, (1 + rot) & 15, S1);
  };
  s16x8 pf[3];
  f32x4 negm[3];
  auto stepAB = [&](Stile& sn, Stile& sc, unsigned ku, unsigned vu, float& lmax, auto DOQK, auto DOSM, auto CINIT) {
    constexpr bool doqk = decltype(DOQK)::value, dosm = decltype(DOSM)::value, cinit = decltype(CINIT)::value;
    s16x8 kf[8], vf[8];
    unsigned kb = ldsb + ku + koff; asm volatile("" : "+v"(kb));
    unsigned vb = ldsb + vu + voff; asm volatile("" : "+v"(vb));
    float e[24];
    if constexpr (doqk) {
      kf[0] = ldsr128A(kb);
      kf[1] = ldsr128A(kb + 4096);
      kf[2] = ldsr128A(kb ^ 64u);
    }
#pragma unroll
    for (int j = 0; j < 8; ++j) {
      if constexpr (doqk) {
        if (j + 3 < 8) kf[j + 3] = ldsr128A((kb ^ (unsigned)(((j + 3) >> 1) << 6)) + ((j + 3) & 1) * 4096);
      }
      if constexpr (dosm) {
        if (j >= 6) { const unsigned a = vb ^ (unsigned)((j - 6) << 5); vf[j - 6] = trread2A(a, a + 4096); }
#pragma unroll
        for (int u = 0; u < 3; ++u) {
          const int q = 3 * j + u, rt = q >> 3, idx = q & 7;
          e[q] = __builtin_amdgcn_exp2f(sc[idx >> 2][rt][idx & 3]);
        }
      }
      if constexpr (doqk) {
#pragma unroll
        for (int rt = 0; rt < 3; ++rt) {
          if (j < 2) sn[j & 1][rt] = mfma16(kf[j], qf[rt][j >> 1], cinit ? negm[rt] : f32x4{0.f, 0.f, 0.f, 0.f});
          else sn[j & 1][rt] = mfma16(kf[j], qf[rt][j >> 1], sn[j & 1][rt]);
        }
      }
      if constexpr (dosm) {
#pragma unroll
        for (int pr = 0; pr < 12; ++pr) {
          const int hi_el = 2 * pr + 1;
          if (hi_el / 3 == j) {
            const int rt = pr >> 2;
            unsigned pk = cvtpk(e[2 * pr], e[2 * pr + 1]);
            u32x4 t4 = __builtin_bit_cast(u32x4, pf[rt]); t4[pr & 3] = pk; pf[rt] = __builtin_bit_cast(s16x8, t4);
          }
        }
      }
      SB();
    }
    if constexpr (dosm) {
      { const unsigned a = vb ^ 64u; vf[2] = trread2A(a, a + 4096); }
      { const unsigned a = vb ^ 96u; vf[3] = trread2A(a, a + 4096); }
      asm volatile("s_nop 7" ::: "memory");
      SB();
#pragma unroll
      for (int rt = 0; rt < 3; ++rt) lacc[rt] = mfma16(ones, pf[rt], lacc[rt]);
      float t0 = 0.f;
#pragma unroll
      for (int c = 0; c < 8; ++c) {
        if (c + 4 < 8) { const unsigned a = vb ^ (unsigned)((c + 4) << 5); vf[c + 4] = trread2A(a, a + 4096); }
#pragma unroll
        for (int rt = 0; rt < 3; ++rt) o[c][rt] = mfma16(vf[c], pf[rt], o[c][rt]);
        if constexpr (doqk) {
          if (c < 6) {
            const int rt = c >> 1, kt = c & 1;
            if (c == 0) t0 = max3a(sn[0][0][0], sn[0][0][1], sn[0][0][2]);
            else t0 = max3a(t0, sn[kt][rt][0], sn[kt][rt][1]);
            if (c == 0) t0 = max2a(t0, sn[0][0][3]); else t0 = max3a(t0, sn[kt][rt][2], sn[kt][rt][3]);
          }
        }
        SB();
      }
      lmax = t0;
    }
  };
  using T_ = std::integral_constant<bool, true>; using F_ = std::integral_constant<bool, false>;
  auto decide = [&](Stile& sn, float lmax) {
    if (__builtin_expect(__any(lmax > THR), 0)) {
#pragma unroll
      for (int rt = 0; rt < 3; ++rt) {
        float a = fmaxf(fmaxf(sn[0][rt][0], sn[0][rt][1]), fmaxf(sn[0][rt][2], sn[0][rt][3]));
        float c2 = fmaxf(fmaxf(sn[1][rt][0], sn[1][rt][1]), fmaxf(sn[1][rt][2], sn[1][rt][3]));
        const float dl = fmaxf(xmax16_32(fmaxf(a, c2)), 0.f);
        const float al = __builtin_amdgcn_exp2f(-dl);
        negm[rt] -= dl; lacc[rt] *= al;
        sn[0][rt] -= dl; sn[1][rt] -= dl;
#pragma unroll
        for (int c = 0; c < 8; ++c) o[c][rt] *= al;
      }
    }
  };

  Stile sa, sb;
  float lmax;
  stepAB(sa, sb, uoff(0), 0, lmax, T_{}, F_{}, F_{});
  issue_kv(5); issue_kv(6);
#pragma unroll
  for (int rt = 0; rt < 3; ++rt) {
    float a = fmaxf(fmaxf(sa[0][rt][0], sa[0][rt][1]), fmaxf(sa[0][rt][2], sa[0][rt][3]));
    float c2 = fmaxf(fmaxf(sa[1][rt][0], sa[1][rt][1]), fmaxf(sa[1][rt][2], sa[1][rt][3]));
    const float m0 = xmax16_32(fmaxf(a, c2));
    negm[rt] = f32x4{-m0, -m0, -m0, -m0};
    sa[0][rt] -= m0; sa[1][rt] -= m0;
  }
#define CY(acc) do {} while (0)
#pragma unroll 1
  for (int t = 0; t < 6; t += 2) {
    step_top(t);
    CY(cyT);
    stepAB(sb, sa, uoff(2 * t + 2), uoff(2 * t + 1), lmax, T_{}, T_{}, T_{});
    decide(sb, lmax);
    CY(cyA);
    step_top(t + 1);
    CY(cyT);
    stepAB(sa, sb, uoff(2 * t + 4), uoff(2 * t + 3), lmax, T_{}, T_{}, T_{});
    decide(sa, lmax);
    CY(cyA);
  }
  step_top(6);
  stepAB(sb, sa, uoff(14), uoff(13), lmax, T_{}, T_{}, T_{});
  decide(sb, lmax);
  step_top(7);
  stepAB(sa, sb, 0, uoff(15), lmax, F_{}, T_{}, F_{});

  STAMP(4);
  float mrow[3], lrow[3];
#pragma unroll
  for (int rt = 0; rt < 3; ++rt) { lrow[rt] = lacc[rt][0]; mrow[rt] = -negm[rt][0]; }
  {
    int lane2 = lane; asm volatile("" : "+v"(lane2));
    const int l15b = lane2 & 15, gqb = lane2 >> 4;
    float* stat = (float*)(lds + RG + 6 * UNIT);
    if (gqb == 0) {
#pragma unroll
      for (int rt = 0; rt < 3; ++rt) { stat[((w * 3 + rt) * 16 + l15b) * 2] = mrow[rt]; stat[((w * 3 + rt) * 16 + l15b) * 2 + 1] = lrow[rt]; }
    }
    auto send = [&](f32x4 (&oo)[8][3], int base) {
#pragma unroll
      for (int cp = 0; cp < 2; ++cp)
#pragma unroll
        for (int rt = 0; rt < 3; ++rt) {
          const f32x4 a0 = oo[base + 2 * cp][rt], a1 = oo[base + 2 * cp + 1][rt];
          u32x4 pk = {cvtpk(a0[0], a0[1]), cvtpk(a0[2], a0[3]), cvtpk(a1[0], a1[1]), cvtpk(a1[2], a1[3])};
          *(u32x4*)(lds + XQ + w * 6144 + (cp * 3 + rt) * 1024 + lane2 * 16) = pk;
        }
    };
    if (hh) send(o, 0); else send(o, 4);
    BAR();
    STAMP(13);
    float s1[3], s2[3];
#pragma unroll
    for (int rt = 0; rt < 3; ++rt) {
      const float m2 = stat[(((w ^ 1) * 3 + rt) * 16 + l15b) * 2], l2 = stat[(((w ^ 1) * 3 + rt) * 16 + l15b) * 2 + 1];
      const float mn = fmaxf(mrow[rt], m2);
      const float w1 = __builtin_amdgcn_exp2f(mrow[rt] - mn), w2 = __builtin_amdgcn_exp2f(m2 - mn);
      const float inv = __builtin_amdgcn_rcpf(lrow[rt] * w1 + l2 * w2);
      s1[rt] = w1 * inv; s2[rt] = w2 * inv;
    }
    auto recv = [&](f32x4 (&oo)[8][3], int base) {
      u32x4 r[6];
#pragma unroll
      for (int i = 0; i < 6; ++i) r[i] = *(const u32x4*)(lds + XQ + (w ^ 1) * 6144 + i * 1024 + lane2 * 16);
      asm volatile("s_waitcnt lgkmcnt(0)" : "+v"(r[0]), "+v"(r[1]), "+v"(r[2]), "+v"(r[3]), "+v"(r[4]), "+v"(r[5]) :: "memory");
#pragma unroll
      for (int cp = 0; cp < 2; ++cp)
#pragma unroll
        for (int rt = 0; rt < 3; ++rt) {
#pragma unroll
          for (int h2 = 0; h2 < 2; ++h2) {
            const f32x4 a = oo[base + 2 * cp + h2][rt];
            const u32x4 rr = r[cp * 3 + rt];
            const float v0 = a[0] * s1[rt] + __uint_as_float(rr[2 * h2] << 16) * s2[rt];
            const float v1 = a[1] * s1[rt] + __uint_as_float(rr[2 * h2] & 0xffff0000u) * s2[rt];
            const float v2 = a[2] * s1[rt] + __uint_as_float(rr[2 * h2 + 1] << 16) * s2[rt];
            const float v3 = a[3] * s1[rt] + __uint_as_float(rr[2 * h2 + 1] & 0xffff0000u) * s2[rt];
            const int row = 16 * rt + l15b;
            const int chunk = 2 * (2 * cp + h2) + (gqb >> 1);
            u32x2 pk = {cvtpk(v0, v1), cvtpk(v2, v3)};
            *(u32x2*)(lds + XQ + (w ^ 1) * 6144 + row * 128 + ((chunk ^ ((l15b >> 1) & 7)) * 16) + (gqb & 1) * 8) = pk;
          }
        }
    };
    STAMP(14);
    if (hh) recv(o, 4); else recv(o, 0);
    BAR();
  }

  STAMP(5);
  int lane3 = lane; asm volatile("" : "+v"(lane3));
  calc_wtr(lane3);
  const int l15c = lane3 & 15, gqc = lane3 >> 4;
  auto rd_frag3 = [&](Frag& f, unsigned stage_off, int ks) {
#pragma unroll
    for (int c = 0; c < 4; ++c) f.w[c] = trread2(lds, stage_off + wtr[c], stage_off + wtr[c] + 512);
    const unsigned blk = XQ + (unsigned)((ks >> 1) ^ 1) * 6144;
#pragma unroll
    for (int rt = 0; rt < 3; ++rt) f.x[rt] = ldsr128(lds, blk + (unsigned)((16 * rt + l15c) * 128 + (((4 * (ks & 1) + gqc) ^ ((l15c >> 1) & 7)) * 16)));
  };
#pragma unroll
  for (int c = 0; c < 4; ++c)
#pragma unroll
    for (int rt = 0; rt < 3; ++rt) acc[c][rt] = f32x4{0.f, 0.f, 0.f, 0.f};
  WAIT_V(4);
  rd_frag3(fa, S0, (0 + rot) & 15); pin(fa);
  issue_w(rwo, 10, (2 + rot) & 15, S0);
  WAIT_V(4);
  rd_frag3(fb, S1, (1 + rot) & 15);
  mm(fa);
  pin(fb);
  issue_w(rwo, 10, (3 + rot) & 15, S1);
#define CY3(acc) do {} while (0)
#pragma unroll 1
  for (int i = 2; i < 14; i += 2) {
    WAIT_V(4);
    CY3(cW);
    rd_frag3(fa, S0, (i + rot) & 15);
    mm(fb);
    pin(fa);
    CY3(cC);
    issue_w(rwo, 10, (i + 2 + rot) & 15, S0);
    CY3(cI);
    WAIT_V(4);
    CY3(cW);
    rd_frag3(fb, S1, (i + 1 + rot) & 15);
    mm(fa);
    pin(fb);
    CY3(cC);
    issue_w(rwo, 10, (i + 3 + rot) & 15, S1);
    CY3(cI);
  }
  WAIT_V(4);
  rd_frag3(fa, S0, (14 + rot) & 15);
  mm(fb);
  pin(fa);
  WAIT_V(0);
  rd_frag3(fb, S1, (15 + rot) & 15);
  mm(fa);
  pin(fb);
  mm(fb);

  STAMP(6);
  {
    char* part = p.ws + WS_PART;
    const __amdgpu_buffer_rsrc_t rs = __builtin_amdgcn_make_buffer_rsrc(part + (size_t)grp * 8 * 8 * 6144, 0, 8 * 8 * 6144, 0x00020000);
    const unsigned base = (unsigned)((w * 8 + kvh) * 6144);
#pragma unroll
    for (int cp = 0; cp < 2; ++cp)
#pragma unroll
      for (int rt = 0; rt < 3; ++rt) {
        const f32x4 a0 = acc[2 * cp][rt], a1 = acc[2 * cp + 1][rt];
        auto r0 = __builtin_amdgcn_permlane16_swap(cvtpk(a0[0], a0[1]), cvtpk(a1[0], a1[1]), false, false);
        auto r1 = __builtin_amdgcn_permlane16_swap(cvtpk(a0[2], a0[3]), cvtpk(a1[2], a1[3]), false, false);
        u32x4 pk = {r0[0], r1[0], r0[1], r1[1]};
        const unsigned off = base + (unsigned)((16 * rt + l15c) * 128 + (16 * (2 * cp + (gqc & 1)) + 8 * (gqc >> 1)) * 2);
        __builtin_amdgcn_raw_buffer_store_b128(pk, rs, off, 0, 16);
      }
    WAIT_V(0);
    BAR();
    STAMP(7);
    AS1 unsigned* flags = (AS1 unsigned*)(p.ws + WS_FLAG + (size_t)grp * 8 * 128);
    if (tid == 0) __hip_atomic_store(flags + kvh * 32, epoch, __ATOMIC_RELAXED, __HIP_MEMORY_SCOPE_AGENT);
    if (w == 0) {
      for (unsigned spins = 0; spins < 4000000u; ++spins) {
        unsigned f = epoch;
        if (lane < 8) f = __hip_atomic_load(flags + lane * 32, __ATOMIC_RELAXED, __HIP_MEMORY_SCOPE_AGENT);
        if (__all(f == epoch)) break;
        __builtin_amdgcn_s_sleep(1);
      }
    }
    BAR();
    STAMP(8);
    if (tid == 0) __hip_atomic_store((AS1 unsigned*)(p.ws + WS_EPOCH + grp * 128), epoch, __ATOMIC_RELAXED, __HIP_MEMORY_SCOPE_AGENT);
    char* outp = p.out + (size_t)b * 48 * 2048 + br * 1024 + kvh * 128;
    if (tid < 384) {
      u32x4 v[8];
#pragma unroll
      for (int sidx = 0; sidx < 8; ++sidx) v[sidx] = __builtin_amdgcn_raw_buffer_load_b128(rs, (unsigned)((kvh * 8 + sidx) * 6144 + tid * 16), 0, 16);
      float s[8];
#pragma unroll
      for (int j = 0; j < 8; ++j) s[j] = 0.f;
#pragma unroll
      for (int sidx = 0; sidx < 8; ++sidx)
#pragma unroll
        for (int j = 0; j < 4; ++j) { s[2 * j] += __uint_as_float(v[sidx][j] << 16); s[2 * j + 1] += __uint_as_float(v[sidx][j] & 0xffff0000u); }
      const int row = tid >> 3, c8 = tid & 7;
      u32x4 pk = {cvtpk(s[0], s[1]), cvtpk(s[2], s[3]), cvtpk(s[4], s[5]), cvtpk(s[6], s[7])};
      *(u32x4*)(outp + (size_t)row * 2048 + c8 * 16) = pk;
    }
  }
  STAMP(9);
}

extern "C" void kernel_launch(void* const* d_in, const int* in_sizes, int n_in, void* d_out, int out_size, void* d_ws, size_t ws_size,
                              hipStream_t stream) {
  Params p;
  for (int br = 0; br < 2; ++br) {
    p.x[br] = (const char*)d_in[5 * br + 0];
    p.k[br] = (const char*)d_in[5 * br + 1];
    p.v[br] = (const char*)d_in[5 * br + 2];
    p.wq[br] = (const char*)d_in[5 * br + 3];
    p.wo[br] = (const char*)d_in[5 * br + 4];
  }
  p.out = (char*)d_out;
  p.ws = (char*)d_ws;
  (void)hipFuncSetAttribute((const void*)fused_gqa_kernel, hipFuncAttributeMaxDynamicSharedMemorySize, LDS_BYTES);
  fused_gqa_kernel<<<256, 512, LDS_BYTES, stream>>>(p);
}
```

```cpp
#include <hip/hip_runtime.h>
#include <hip/hip_bf16.h>
#include <stdint.h>
#include <type_traits>

typedef short s16x4 __attribute__((ext_vector_type(4)));
typedef short s16x8 __attribute__((ext_vector_type(8)));
typedef __bf16 bf16x8 __attribute__((ext_vector_type(8)));
typedef __bf16 bf16x2 __attribute__((ext_vector_type(2)));
typedef float f32x4 __attribute__((ext_vector_type(4)));
typedef float f32x2 __attribute__((ext_vector_type(2)));
typedef unsigned u32x4 __attribute__((ext_vector_type(4)));
typedef unsigned u32x2 __attribute__((ext_vector_type(2)));

#define AS1 __attribute__((address_space(1)))
#define AS3 __attribute__((address_space(3)))
#define DI __device__ __forceinline__

struct Params {
  const char* x[2];
  const char* k[2];
  const char* v[2];
  const char* wq[2];
  const char* wo[2];
  char* out;
  char* ws;
};

constexpr unsigned XQ = 0;
constexpr unsigned RG = 49152;
constexpr unsigned UNIT = 16384;
constexpr int LDS_BYTES = 163840;

constexpr size_t WS_EPOCH = 0;
constexpr size_t WS_FLAG = 8192;
constexpr size_t WS_PART = 262144;

constexpr float CEXP = 0.08838834764831845f * 1.4426950408889634f;
constexpr float THR = 8.0f;

#define WAIT_V(n) asm volatile("s_waitcnt vmcnt(" #n ")" ::: "memory")
#define BAR() do { asm volatile("s_waitcnt lgkmcnt(0)" ::: "memory"); __builtin_amdgcn_s_barrier(); asm volatile("" ::: "memory"); } while (0)
#define STAMP(i) do {} while (0)

DI f32x4 mfma16(s16x8 a, s16x8 b, f32x4 c) {
  return __builtin_amdgcn_mfma_f32_16x16x32_bf16(__builtin_bit_cast(bf16x8, a), __builtin_bit_cast(bf16x8, b), c, 0, 0, 0);
}
DI unsigned cvtpk(float lo, float hi) {
  f32x2 v = {lo, hi};
  bf16x2 b = __builtin_convertvector(v, bf16x2);
  return __builtin_bit_cast(unsigned, b);
}
DI s16x4 trread(const char* lds, unsigned off) {
  return __builtin_amdgcn_ds_read_tr16_b64_v4i16((AS3 s16x4*)(lds + off));
}
DI s16x4 trreadA(unsigned a) { return __builtin_amdgcn_ds_read_tr16_b64_v4i16((AS3 s16x4*)(size_t)a); }
DI s16x8 trread2A(unsigned a0, unsigned a1) { s16x4 a = trreadA(a0), b = trreadA(a1); return __builtin_shufflevector(a, b, 0, 1, 2, 3, 4, 5, 6, 7); }
DI s16x8 ldsr128A(unsigned a) { return *(const AS3 s16x8*)(size_t)a; }
DI s16x8 trread2(const char* lds, unsigned off0, unsigned off1) {
  s16x4 a = trread(lds, off0), b = trread(lds, off1);
  return __builtin_shufflevector(a, b, 0, 1, 2, 3, 4, 5, 6, 7);
}
DI s16x8 ldsr128(const char* lds, unsigned off) { return *(const s16x8*)(lds + off); }
DI void dma_d(__amdgpu_buffer_rsrc_t rs, unsigned voff, unsigned soff, unsigned ldsaddr) {
  asm volatile("s_mov_b32 m0, %0\n\ts_nop 0\n\tbuffer_load_dwordx4 %1, %2, %3 offen lds" ::"s"(ldsaddr), "v"(voff), "s"(rs), "s"(soff) : "memory");
}
DI void dma_nt(__amdgpu_buffer_rsrc_t rs, unsigned voff, unsigned soff, unsigned ldsaddr) {
  asm volatile("s_mov_b32 m0, %0\n\ts_nop 0\n\tbuffer_load_dwordx4 %1, %2, %3 offen nt lds" ::"s"(ldsaddr), "v"(voff), "s"(rs), "s"(soff) : "memory");
}
DI __amdgpu_buffer_rsrc_t mkrsrc(const void* p, unsigned bytes) {
  return __builtin_amdgcn_make_buffer_rsrc((void*)p, 0, bytes, 0x00020000);
}
DI float xmax16_32(float v) {
  unsigned u = __float_as_uint(v);
  auto r = __builtin_amdgcn_permlane16_swap(u, u, false, false);
  v = fmaxf(__uint_as_float(r[0]), __uint_as_float(r[1]));
  u = __float_as_uint(v);
  auto r2 = __builtin_amdgcn_permlane32_swap(u, u, false, false);
  return fmaxf(__uint_as_float(r2[0]), __uint_as_float(r2[1]));
}
DI float xsum16_32(float v) {
  unsigned u = __float_as_uint(v);
  auto r = __builtin_amdgcn_permlane16_swap(u, u, false, false);
  v = __uint_as_float(r[0]) + __uint_as_float(r[1]);
  u = __float_as_uint(v);
  auto r2 = __builtin_amdgcn_permlane32_swap(u, u, false, false);
  return __uint_as_float(r2[0]) + __uint_as_float(r2[1]);
}

__global__ __launch_bounds__(512, 2) void fused_gqa_kernel(Params p) {
  extern __shared__ __attribute__((aligned(16))) char lds[];
  const int tid = threadIdx.x, lane = tid & 63;
  const int w = __builtin_amdgcn_readfirstlane(tid >> 6);
  const int bid = blockIdx.x;
  const int kvh = bid & 7, b = (bid >> 3) & 15, br = bid >> 7;
  const int grp = bid >> 3;
  const int l15 = lane & 15, gq = lane >> 4;
  const int tq = (lane & 15) >> 2, tp = lane & 3;
  const int g = w >> 1, hh = w & 1;
  const unsigned ldsb = (unsigned)(uintptr_t)lds;

  STAMP(0);
  const __amdgpu_buffer_rsrc_t rx = mkrsrc(p.x[br] + (size_t)b * 48 * 1024, 48 * 1024);
  const __amdgpu_buffer_rsrc_t rk = mkrsrc(p.k[br] + (size_t)(b * 8 + kvh) * 131072, 131072);
  const __amdgpu_buffer_rsrc_t rv = mkrsrc(p.v[br] + (size_t)(b * 8 + kvh) * 131072, 131072);
  const __amdgpu_buffer_rsrc_t rwq = mkrsrc(p.wq[br] + kvh * 1024, 512 * 8192 - kvh * 1024);
  const __amdgpu_buffer_rsrc_t rwo = mkrsrc(p.wo[br] + (size_t)kvh * 512 * 1024, 512 * 1024);

  const int wr = lane >> 3, wpos = lane & 7;
  const unsigned wsw0 = (unsigned)(wpos ^ (((wr >> 1) & 1) << 1)) * 16 + w * 128;

  unsigned wv0 = ((unsigned)wr << 13) + wsw0;
  auto issue_w = [&](__amdgpu_buffer_rsrc_t rs, int lgstride, int ks, unsigned stage_off) {
    const unsigned v0 = wv0, v1 = wv0 ^ 64;
#pragma unroll
    for (int i = 0; i < 4; ++i)
      dma_d(rs, (i & 1) ? v1 : v0, (unsigned)(ks * 32 + i * 8) << lgstride, ldsb + stage_off + w * 4096 + i * 1024);
  };
  const int kr = lane >> 4, kpos = lane & 15;
  unsigned kvo[2], vvo[2];
#pragma unroll
  for (int i = 0; i < 2; ++i) {
    const int row = (2 * w + i) * 4 + kr;
    kvo[i] = (unsigned)(row * 256 + ((kpos ^ (row & 15)) * 16));
    vvo[i] = (unsigned)(row * 256 + ((kpos ^ ((row & 7) << 1)) * 16));
  }
  auto issue_kv = [&](int n) {
    const unsigned dst = ldsb + RG + (unsigned)((4 + n) % 7) * UNIT + 2 * w * 1024;
    const unsigned so = (unsigned)(n >> 1) * 16384;
    if (n & 1) { dma_nt(rv, vvo[0], so, dst); dma_nt(rv, vvo[1], so, dst + 1024); }
    else       { dma_nt(rk, kvo[0], so, dst); dma_nt(rk, kvo[1], so, dst + 1024); }
  };

  const int rot = 2 * (b & 7);
  const unsigned xvo = (unsigned)((lane >> 3) * 1024 + (((lane & 7) ^ (((lane >> 3) >> 1) & 3)) * 16));
  auto issue_x = [&](int q) {
    const int kbi = q / 6, j = q % 6;
    const int kb = ((rot >> 1) + kbi) & 7;
    dma_d(rx, xvo ^ (unsigned)((j & 1) << 6), (unsigned)(j * 8 * 1024 + kb * 128), ldsb + XQ + kb * 6144 + j * 1024);
  };
  issue_x(w);
  issue_w(rwq, 13, (0 + rot) & 15, RG);
  issue_w(rwq, 13, (1 + rot) & 15, RG + 2 * UNIT);
#pragma unroll
  for (int i = 0; i < 5; ++i) issue_x(8 + w + 8 * i);
  STAMP(12);
  const unsigned epoch = __hip_atomic_load((const AS1 unsigned*)(p.ws + WS_EPOCH + grp * 128), __ATOMIC_RELAXED, __HIP_MEMORY_SCOPE_AGENT) + 1u;

  unsigned wtr[4];
  auto calc_wtr = [&](int ln) {
    const int gq_ = ln >> 4, tq_ = (ln & 15) >> 2, tp_ = ln & 3;
    const int wf = (((tq_ >> 1) & 1) << 1) | ((gq_ & 1) << 2);
#pragma unroll
    for (int c = 0; c < 4; ++c) wtr[c] = (unsigned)((8 * gq_ + tq_) * 128 + (((2 * c + (tp_ >> 1)) ^ wf) * 16) + 8 * (tp_ & 1)) + w * 4096;
  };
  calc_wtr(lane);

  struct Frag { s16x8 w[4]; s16x8 x[3]; };
  auto rd_frag1 = [&](Frag& f, unsigned stage_off, int ks) {
#pragma unroll
    for (int c = 0; c < 4; ++c) f.w[c] = trread2(lds, stage_off + wtr[c], stage_off + wtr[c] + 512);
#pragma unroll
    for (int rt = 0; rt < 3; ++rt) f.x[rt] = ldsr128(lds, XQ + (unsigned)(ks >> 1) * 6144 + (unsigned)((16 * rt + l15) * 128 + (((4 * (ks & 1) + gq) ^ ((l15 >> 1) & 7)) * 16)));
  };
  auto pin = [&](Frag& f) {
    asm volatile("s_waitcnt lgkmcnt(0)" : "+v"(f.w[0]), "+v"(f.w[1]), "+v"(f.w[2]), "+v"(f.w[3]), "+v"(f.x[0]), "+v"(f.x[1]), "+v"(f.x[2])::"memory");
  };
  f32x4 acc[4][3];
  auto mm = [&](Frag& f) {
#pragma unroll
    for (int c = 0; c < 4; ++c)
#pragma unroll
      for (int rt = 0; rt < 3; ++rt) acc[c][rt] = mfma16(f.w[c], f.x[rt], acc[c][rt]);
  };

#pragma unroll
  for (int c = 0; c < 4; ++c)
#pragma unroll
    for (int rt = 0; rt < 3; ++rt) acc[c][rt] = f32x4{0.f, 0.f, 0.f, 0.f};
  constexpr unsigned S0 = RG, S1 = RG + 2 * UNIT;
  Frag fa, fb;
  WAIT_V(10); BAR();
  STAMP(1);
  rd_frag1(fa, S0, (0 + rot) & 15); pin(fa);
  issue_w(rwq, 13, (2 + rot) & 15, S0);
  WAIT_V(10);
  rd_frag1(fb, S1, (1 + rot) & 15);
  mm(fa);
  pin(fb);
  issue_w(rwq, 13, (3 + rot) & 15, S1);
  issue_kv(0); issue_kv(1); issue_kv(2);
  WAIT_V(10); BAR();
  rd_frag1(fa, S0, (2 + rot) & 15);
  mm(fb);
  pin(fa);
  issue_w(rwq, 13, (4 + rot) & 15, S0);
  WAIT_V(10);
  rd_frag1(fb, S1, (3 + rot) & 15);
  mm(fa);
  pin(fb);
  issue_w(rwq, 13, (5 + rot) & 15, S1);
#define CY1(acc) do {} while (0)
#pragma unroll 1
  for (int i = 4; i < 14; i += 2) {
    WAIT_V(4);
    CY1(cW);
    rd_frag1(fa, S0, (i + rot) & 15);
    mm(fb);
    pin(fa);
    CY1(cC);
    issue_w(rwq, 13, (i + 2 + rot) & 15, S0);
    CY1(cI);
    WAIT_V(4);
    CY1(cW);
    rd_frag1(fb, S1, (i + 1 + rot) & 15);
    mm(fa);
    pin(fb);
    CY1(cC);
    issue_w(rwq, 13, (i + 3 + rot) & 15, S1);
    CY1(cI);
  }
  WAIT_V(4);
  rd_frag1(fa, S0, (14 + rot) & 15);
  mm(fb);
  pin(fa);
  WAIT_V(0);
  rd_frag1(fb, S1, (15 + rot) & 15);
  mm(fa);
  pin(fb);
  mm(fb);
  BAR();
  STAMP(2);
#pragma unroll
  for (int c = 0; c < 4; ++c)
#pragma unroll
    for (int rt = 0; rt < 3; ++rt) {
      const int row = 16 * rt + l15;
      const int chunk = 8 * hh + 2 * c + (gq >> 1);
      u32x2 v = {cvtpk(acc[c][rt][0] * CEXP, acc[c][rt][1] * CEXP), cvtpk(acc[c][rt][2] * CEXP, acc[c][rt][3] * CEXP)};
      *(u32x2*)(lds + XQ + g * 12288 + row * 256 + ((chunk ^ l15) * 16) + (gq & 1) * 8) = v;
    }
  BAR();
  s16x8 qf[3][4];
#pragma unroll
  for (int rt = 0; rt < 3; ++rt)
#pragma unroll
    for (int s = 0; s < 4; ++s) qf[rt][s] = ldsr128(lds, XQ + g * 12288 + (16 * rt + l15) * 256 + (((4 * s + gq) ^ l15) * 16));
  issue_kv(3); issue_kv(4);

  wv0 = (wv0 & 8191u) + ((wv0 >> 13) << 10);
  STAMP(3);
  f32x4 o[8][3];
#pragma unroll
  for (int c = 0; c < 8; ++c)
#pragma unroll
    for (int rt = 0; rt < 3; ++rt) o[c][rt] = f32x4{0.f, 0.f, 0.f, 0.f};
  f32x4 lacc[3];
#pragma unroll
  for (int rt = 0; rt < 3; ++rt) lacc[rt] = f32x4{0.f, 0.f, 0.f, 0.f};
  const s16x8 ones = {0x3F80, 0x3F80, 0x3F80, 0x3F80, 0x3F80, 0x3F80, 0x3F80, 0x3F80};
  const unsigned koff = (unsigned)((32 * hh + l15) * 256 + ((gq ^ l15) * 16));
  const int vrow = 32 * hh + 4 * gq + tq;
  const int vsw = (vrow & 7) << 1;
  const unsigned voff = (unsigned)(vrow * 256 + (((tp >> 1) ^ vsw) * 16) + 8 * (tp & 1));

  typedef f32x4 Stile[2][3];
#define SB() __builtin_amdgcn_sched_barrier(0)
  auto max3a = [](float a, float b2, float c) { float r; asm("v_max3_f32 %0, %1, %2, %3" : "=v"(r) : "v"(a), "v"(b2), "v"(c)); return r; };
  auto max2a = [](float a, float b2) { float r; asm("v_max_f32_e32 %0, %1, %2" : "=v"(r) : "v"(a), "v"(b2)); return r; };
  auto xmax = [&](float v) {
    unsigned u = __float_as_uint(v);
    auto r = __builtin_amdgcn_permlane16_swap(u, u, false, false);
    v = max2a(__uint_as_float(r[0]), __uint_as_float(r[1]));
    u = __float_as_uint(v);
    auto r2 = __builtin_amdgcn_permlane32_swap(u, u, false, false);
    return max2a(__uint_as_float(r2[0]), __uint_as_float(r2[1]));
  };
  auto uoff = [&](int n) { return RG + (unsigned)((4 + n) % 7) * UNIT; };
  auto step_top = [&](int t) {
    if (t == 0) WAIT_V(8);
    else if (t <= 5) WAIT_V(6);
    else if (t == 6) WAIT_V(2);
    else WAIT_V(4);
    BAR();
    if (t == 0) issue_kv(7);
    else if (t <= 4) { issue_kv(2 * t + 6); issue_kv(2 * t + 7); }
    else if (t == 6) issue_w(rwo, 10, (0 + rot) & 15, S0);
    else if (t == 7) issue_w(rwo, 10, (1 + rot) & 15, S1);
  };
  s16x8 pf[3];
  f32x4 negm[3];
  auto stepAB = [&](Stile& sn, Stile& sc, unsigned ku, unsigned vu, float& lmax, auto DOQK, auto DOSM, auto CINIT) {
    constexpr bool doqk = decltype(DOQK)::value, dosm = decltype(DOSM)::value, cinit = decltype(CINIT)::value;
    s16x8 kf[8], vf[8];
    unsigned kb = ldsb + ku + koff; asm volatile("" : "+v"(kb));
    unsigned vb = ldsb + vu + voff; asm volatile("" : "+v"(vb));
    float e[24];
    if constexpr (doqk) {
      kf[0] = ldsr128A(kb);
      kf[1] = ldsr128A(kb + 4096);
      kf[2] = ldsr128A(kb ^ 64u);
    }
#pragma unroll
    for (int j = 0; j < 8; ++j) {
      if constexpr (doqk) {
        if (j + 3 < 8) kf[j + 3] = ldsr128A((kb ^ (unsigned)(((j + 3) >> 1) << 6)) + ((j + 3) & 1) * 4096);
      }
      if constexpr (dosm) {
        if (j >= 6) { const unsigned a = vb ^ (unsigned)((j - 6) << 5); vf[j - 6] = trread2A(a, a + 4096); }
#pragma unroll
        for (int u = 0; u < 3; ++u) {
          const int q = 3 * j + u, rt = q >> 3, idx = q & 7;
          e[q] = __builtin_amdgcn_exp2f(sc[idx >> 2][rt][idx & 3]);
        }
      }
      if constexpr (doqk) {
#pragma unroll
        for (int rt = 0; rt < 3; ++rt) {
          if (j < 2) sn[j & 1][rt] = mfma16(kf[j], qf[rt][j >> 1], cinit ? negm[rt] : f32x4{0.f, 0.f, 0.f, 0.f});
          else sn[j & 1][rt] = mfma16(kf[j], qf[rt][j >> 1], sn[j & 1][rt]);
        }
      }
      if constexpr (dosm) {
#pragma unroll
        for (int pr = 0; pr < 12; ++pr) {
          const int hi_el = 2 * pr + 1;
          if (hi_el / 3 == j) {
            const int rt = pr >> 2;
            unsigned pk = cvtpk(e[2 * pr], e[2 * pr + 1]);
            u32x4 t4 = __builtin_bit_cast(u32x4, pf[rt]); t4[pr & 3] = pk; pf[rt] = __builtin_bit_cast(s16x8, t4);
          }
        }
      }
      SB();
    }
    if constexpr (dosm) {
      { const unsigned a = vb ^ 64u; vf[2] = trread2A(a, a + 4096); }
      { const unsigned a = vb ^ 96u; vf[3] = trread2A(a, a + 4096); }
      asm volatile("s_nop 7" ::: "memory");
      SB();
#pragma unroll
      for (int rt = 0; rt < 3; ++rt) lacc[rt] = mfma16(ones, pf[rt], lacc[rt]);
      float t0 = 0.f;
#pragma unroll
      for (int c = 0; c < 8; ++c) {
        if (c + 4 < 8) { const unsigned a = vb ^ (unsigned)((c + 4) << 5); vf[c + 4] = trread2A(a, a + 4096); }
#pragma unroll
        for (int rt = 0; rt < 3; ++rt) o[c][rt] = mfma16(vf[c], pf[rt], o[c][rt]);
        if constexpr (doqk) {
          if (c < 6) {
            const int rt = c >> 1, kt = c & 1;
            if (c == 0) t0 = max3a(sn[0][0][0], sn[0][0][1], sn[0][0][2]);
            else t0 = max3a(t0, sn[kt][rt][0], sn[kt][rt][1]);
            if (c == 0) t0 = max2a(t0, sn[0][0][3]); else t0 = max3a(t0, sn[kt][rt][2], sn[kt][rt][3]);
          }
        }
        SB();
      }
      lmax = t0;
    }
  };
  using T_ = std::integral_constant<bool, true>; using F_ = std::integral_constant<bool, false>;
  auto decide = [&](Stile& sn, float lmax) {
    if (__builtin_expect(__any(lmax > THR), 0)) {
#pragma unroll
      for (int rt = 0; rt < 3; ++rt) {
        float a = fmaxf(fmaxf(sn[0][rt][0], sn[0][rt][1]), fmaxf(sn[0][rt][2], sn[0][rt][3]));
        float c2 = fmaxf(fmaxf(sn[1][rt][0], sn[1][rt][1]), fmaxf(sn[1][rt][2], sn[1][rt][3]));
        const float dl = fmaxf(xmax16_32(fmaxf(a, c2)), 0.f);
        const float al = __builtin_amdgcn_exp2f(-dl);
        negm[rt] -= dl; lacc[rt] *= al;
        sn[0][rt] -= dl; sn[1][rt] -= dl;
#pragma unroll
        for (int c = 0; c < 8; ++c) o[c][rt] *= al;
      }
    }
  };

  Stile sa, sb;
  float lmax;
  stepAB(sa, sb, uoff(0), 0, lmax, T_{}, F_{}, F_{});
  issue_kv(5); issue_kv(6);
#pragma unroll
  for (int rt = 0; rt < 3; ++rt) {
    float a = fmaxf(fmaxf(sa[0][rt][0], sa[0][rt][1]), fmaxf(sa[0][rt][2], sa[0][rt][3]));
    float c2 = fmaxf(fmaxf(sa[1][rt][0], sa[1][rt][1]), fmaxf(sa[1][rt][2], sa[1][rt][3]));
    const float m0 = xmax16_32(fmaxf(a, c2));
    negm[rt] = f32x4{-m0, -m0, -m0, -m0};
    sa[0][rt] -= m0; sa[1][rt] -= m0;
  }
#define CY(acc) do {} while (0)
#pragma unroll 1
  for (int t = 0; t < 6; t += 2) {
    step_top(t);
    CY(cyT);
    stepAB(sb, sa, uoff(2 * t + 2), uoff(2 * t + 1), lmax, T_{}, T_{}, T_{});
    decide(sb, lmax);
    CY(cyA);
    step_top(t + 1);
    CY(cyT);
    stepAB(sa, sb, uoff(2 * t + 4), uoff(2 * t + 3), lmax, T_{}, T_{}, T_{});
    decide(sa, lmax);
    CY(cyA);
  }
  step_top(6);
  stepAB(sb, sa, uoff(14), uoff(13), lmax, T_{}, T_{}, T_{});
  decide(sb, lmax);
  step_top(7);
  stepAB(sa, sb, 0, uoff(15), lmax, F_{}, T_{}, F_{});

  STAMP(4);
  float mrow[3], lrow[3];
#pragma unroll
  for (int rt = 0; rt < 3; ++rt) { lrow[rt] = lacc[rt][0]; mrow[rt] = -negm[rt][0]; }
  {
    int lane2 = lane; asm volatile("" : "+v"(lane2));
    const int l15b = lane2 & 15, gqb = lane2 >> 4;
    float* stat = (float*)(lds + RG + 6 * UNIT);
    if (gqb == 0) {
#pragma unroll
      for (int rt = 0; rt < 3; ++rt) { stat[((w * 3 + rt) * 16 + l15b) * 2] = mrow[rt]; stat[((w * 3 + rt) * 16 + l15b) * 2 + 1] = lrow[rt]; }
    }
    auto send = [&](f32x4 (&oo)[8][3], int base) {
#pragma unroll
      for (int cp = 0; cp < 2; ++cp)
#pragma unroll
        for (int rt = 0; rt < 3; ++rt) {
          const f32x4 a0 = oo[base + 2 * cp][rt], a1 = oo[base + 2 * cp + 1][rt];
          u32x4 pk = {cvtpk(a0[0], a0[1]), cvtpk(a0[2], a0[3]), cvtpk(a1[0], a1[1]), cvtpk(a1[2], a1[3])};
          *(u32x4*)(lds + XQ + w * 6144 + (cp * 3 + rt) * 1024 + lane2 * 16) = pk;
        }
    };
    if (hh) send(o, 0); else send(o, 4);
    BAR();
    STAMP(13);
    float s1[3], s2[3];
#pragma unroll
    for (int rt = 0; rt < 3; ++rt) {
      const float m2 = stat[(((w ^ 1) * 3 + rt) * 16 + l15b) * 2], l2 = stat[(((w ^ 1) * 3 + rt) * 16 + l15b) * 2 + 1];
      const float mn = fmaxf(mrow[rt], m2);
      const float w1 = __builtin_amdgcn_exp2f(mrow[rt] - mn), w2 = __builtin_amdgcn_exp2f(m2 - mn);
      const float inv = __builtin_amdgcn_rcpf(lrow[rt] * w1 + l2 * w2);
      s1[rt] = w1 * inv; s2[rt] = w2 * inv;
    }
    auto recv = [&](f32x4 (&oo)[8][3], int base) {
      u32x4 r[6];
#pragma unroll
      for (int i = 0; i < 6; ++i) r[i] = *(const u32x4*)(lds + XQ + (w ^ 1) * 6144 + i * 1024 + lane2 * 16);
      asm volatile("s_waitcnt lgkmcnt(0)" : "+v"(r[0]), "+v"(r[1]), "+v"(r[2]), "+v"(r[3]), "+v"(r[4]), "+v"(r[5]) :: "memory");
#pragma unroll
      for (int cp = 0; cp < 2; ++cp)
#pragma unroll
        for (int rt = 0; rt < 3; ++rt) {
#pragma unroll
          for (int h2 = 0; h2 < 2; ++h2) {
            const f32x4 a = oo[base + 2 * cp + h2][rt];
            const u32x4 rr = r[cp * 3 + rt];
            const float v0 = a[0] * s1[rt] + __uint_as_float(rr[2 * h2] << 16) * s2[rt];
            const float v1 = a[1] * s1[rt] + __uint_as_float(rr[2 * h2] & 0xffff0000u) * s2[rt];
            const float v2 = a[2] * s1[rt] + __uint_as_float(rr[2 * h2 + 1] << 16) * s2[rt];
            const float v3 = a[3] * s1[rt] + __uint_as_float(rr[2 * h2 + 1] & 0xffff0000u) * s2[rt];
            const int row = 16 * rt + l15b;
            const int chunk = 2 * (2 * cp + h2) + (gqb >> 1);
            u32x2 pk = {cvtpk(v0, v1), cvtpk(v2, v3)};
            *(u32x2*)(lds + XQ + (w ^ 1) * 6144 + row * 128 + ((chunk ^ ((l15b >> 1) & 7)) * 16) + (gqb & 1) * 8) = pk;
          }
        }
    };
    STAMP(14);
    if (hh) recv(o, 4); else recv(o, 0);
    BAR();
  }

  STAMP(5);
  int lane3 = lane; asm volatile("" : "+v"(lane3));
  calc_wtr(lane3);
  const int l15c = lane3 & 15, gqc = lane3 >> 4;
  auto rd_frag3 = [&](Frag& f, unsigned stage_off, int ks) {
#pragma unroll
    for (int c = 0; c < 4; ++c) f.w[c] = trread2(lds, stage_off + wtr[c], stage_off + wtr[c] + 512);
    const unsigned blk = XQ + (unsigned)((ks >> 1) ^ 1) * 6144;
#pragma unroll
    for (int rt = 0; rt < 3; ++rt) f.x[rt] = ldsr128(lds, blk + (unsigned)((16 * rt + l15c) * 128 + (((4 * (ks & 1) + gqc) ^ ((l15c >> 1) & 7)) * 16)));
  };
#pragma unroll
  for (int c = 0; c < 4; ++c)
#pragma unroll
    for (int rt = 0; rt < 3; ++rt) acc[c][rt] = f32x4{0.f, 0.f, 0.f, 0.f};
  WAIT_V(4);
  rd_frag3(fa, S0, (0 + rot) & 15); pin(fa);
  issue_w(rwo, 10, (2 + rot) & 15, S0);
  WAIT_V(4);
  rd_frag3(fb, S1, (1 + rot) & 15);
  mm(fa);
  pin(fb);
  issue_w(rwo, 10, (3 + rot) & 15, S1);
#define CY3(acc) do {} while (0)
#pragma unroll 1
  for (int i = 2; i < 14; i += 2) {
    WAIT_V(4);
    CY3(cW);
    rd_frag3(fa, S0, (i + rot) & 15);
    mm(fb);
    pin(fa);
    CY3(cC);
    issue_w(rwo, 10, (i + 2 + rot) & 15, S0);
    CY3(cI);
    WAIT_V(4);
    CY3(cW);
    rd_frag3(fb, S1, (i + 1 + rot) & 15);
    mm(fa);
    pin(fb);
    CY3(cC);
    issue_w(rwo, 10, (i + 3 + rot) & 15, S1);
    CY3(cI);
  }
  WAIT_V(4);
  rd_frag3(fa, S0, (14 + rot) & 15);
  mm(fb);
  pin(fa);
  WAIT_V(0);
  rd_frag3(fb, S1, (15 + rot) & 15);
  mm(fa);
  pin(fb);
  mm(fb);

  STAMP(6);
  {
    char* part = p.ws + WS_PART;
    const __amdgpu_buffer_rsrc_t rs = __builtin_amdgcn_make_buffer_rsrc(part + (size_t)grp * 8 * 8 * 6144, 0, 8 * 8 * 6144, 0x00020000);
    const unsigned base = (unsigned)((w * 8 + kvh) * 6144);
#pragma unroll
    for (int cp = 0; cp < 2; ++cp)
#pragma unroll
      for (int rt = 0; rt < 3; ++rt) {
        const f32x4 a0 = acc[2 * cp][rt], a1 = acc[2 * cp + 1][rt];
        auto r0 = __builtin_amdgcn_permlane16_swap(cvtpk(a0[0], a0[1]), cvtpk(a1[0], a1[1]), false, false);
        auto r1 = __builtin_amdgcn_permlane16_swap(cvtpk(a0[2], a0[3]), cvtpk(a1[2], a1[3]), false, false);
        u32x4 pk = {r0[0], r1[0], r0[1], r1[1]};
        const unsigned off = base + (unsigned)((16 * rt + l15c) * 128 + (16 * (2 * cp + (gqc & 1)) + 8 * (gqc >> 1)) * 2);
        __builtin_amdgcn_raw_buffer_store_b128(pk, rs, off, 0, 16);
      }
    WAIT_V(0);
    STAMP(7);
    AS1 unsigned* flags = (AS1 unsigned*)(p.ws + WS_FLAG + (size_t)grp * 64 * 64);
    if (lane == 0) __hip_atomic_store(flags + (w * 8 + kvh) * 16, epoch, __ATOMIC_RELAXED, __HIP_MEMORY_SCOPE_AGENT);
    for (unsigned spins = 0; spins < 4000000u; ++spins) {
      unsigned f = epoch;
      if (lane < 8) f = __hip_atomic_load(flags + (kvh * 8 + lane) * 16, __ATOMIC_RELAXED, __HIP_MEMORY_SCOPE_AGENT);
      if (__all(f == epoch)) break;
      __builtin_amdgcn_s_sleep(1);
    }
    asm volatile("" ::: "memory");
    STAMP(8);
    if (tid == 0) __hip_atomic_store((AS1 unsigned*)(p.ws + WS_EPOCH + grp * 128), epoch, __ATOMIC_RELAXED, __HIP_MEMORY_SCOPE_AGENT);
    char* outp = p.out + (size_t)b * 48 * 2048 + br * 1024 + kvh * 128;
    const int q8 = w * 48 + lane;
    if (lane < 48) {
      u32x4 v[8];
#pragma unroll
      for (int sidx = 0; sidx < 8; ++sidx) v[sidx] = __builtin_amdgcn_raw_buffer_load_b128(rs, (unsigned)((kvh * 8 + sidx) * 6144 + q8 * 16), 0, 16);
      float s[8];
#pragma unroll
      for (int j = 0; j < 8; ++j) s[j] = 0.f;
#pragma unroll
      for (int sidx = 0; sidx < 8; ++sidx)
#pragma unroll
        for (int j = 0; j < 4; ++j) { s[2 * j] += __uint_as_float(v[sidx][j] << 16); s[2 * j + 1] += __uint_as_float(v[sidx][j] & 0xffff0000u); }
      const int row = q8 >> 3, c8 = q8 & 7;
      u32x4 pk = {cvtpk(s[0], s[1]), cvtpk(s[2], s[3]), cvtpk(s[4], s[5]), cvtpk(s[6], s[7])};
      *(u32x4*)(outp + (size_t)row * 2048 + c8 * 16) = pk;
    }
  }
  STAMP(9);
}

extern "C" void kernel_launch(void* const* d_in, const int* in_sizes, int n_in, void* d_out, int out_size, void* d_ws, size_t ws_size,
                              hipStream_t stream) {
  Params p;
  for (int br = 0; br < 2; ++br) {
    p.x[br] = (const char*)d_in[5 * br + 0];
    p.k[br] = (const char*)d_in[5 * br + 1];
    p.v[br] = (const char*)d_in[5 * br + 2];
    p.wq[br] = (const char*)d_in[5 * br + 3];
    p.wo[br] = (const char*)d_in[5 * br + 4];
  }
  p.out = (char*)d_out;
  p.ws = (char*)d_ws;
  (void)hipFuncSetAttribute((const void*)fused_gqa_kernel, hipFuncAttributeMaxDynamicSharedMemorySize, LDS_BYTES);
  fused_gqa_kernel<<<256, 512, LDS_BYTES, stream>>>(p);
}
```

```cpp
#include <hip/hip_runtime.h>
#include <hip/hip_bf16.h>
#include <stdint.h>
#include <type_traits>

typedef short s16x4 __attribute__((ext_vector_type(4)));
typedef short s16x8 __attribute__((ext_vector_type(8)));
typedef __bf16 bf16x8 __attribute__((ext_vector_type(8)));
typedef __bf16 bf16x2 __attribute__((ext_vector_type(2)));
typedef float f32x4 __attribute__((ext_vector_type(4)));
typedef float f32x2 __attribute__((ext_vector_type(2)));
typedef unsigned u32x4 __attribute__((ext_vector_type(4)));
typedef unsigned u32x2 __attribute__((ext_vector_type(2)));

#define AS1 __attribute__((address_space(1)))
#define AS3 __attribute__((address_space(3)))
#define DI __device__ __forceinline__

struct Params {
  const char* x[2];
  const char* k[2];
  const char* v[2];
  const char* wq[2];
  const char* wo[2];
  char* out;
  char* ws;
};

constexpr unsigned XQ = 0;
constexpr unsigned RG = 49152;
constexpr unsigned UNIT = 16384;
constexpr int LDS_BYTES = 163840;

constexpr size_t WS_EPOCH = 0;
constexpr size_t WS_FLAG = 8192;
constexpr size_t WS_PART = 262144;

constexpr float CEXP = 0.08838834764831845f * 1.4426950408889634f;
constexpr float THR = 8.0f;

#define WAIT_V(n) asm volatile("s_waitcnt vmcnt(" #n ")" ::: "memory")
#define BAR() do { asm volatile("s_waitcnt lgkmcnt(0)" ::: "memory"); __builtin_amdgcn_s_barrier(); asm volatile("" ::: "memory"); } while (0)
#define STAMP(i) do {} while (0)

DI f32x4 mfma16(s16x8 a, s16x8 b, f32x4 c) {
  return __builtin_amdgcn_mfma_f32_16x16x32_bf16(__builtin_bit_cast(bf16x8, a), __builtin_bit_cast(bf16x8, b), c, 0, 0, 0);
}
DI unsigned cvtpk(float lo, float hi) {
  f32x2 v = {lo, hi};
  bf16x2 b = __builtin_convertvector(v, bf16x2);
  return __builtin_bit_cast(unsigned, b);
}
DI s16x4 trread(const char* lds, unsigned off) {
  return __builtin_amdgcn_ds_read_tr16_b64_v4i16((AS3 s16x4*)(lds + off));
}
DI s16x4 trreadA(unsigned a) { return __builtin_amdgcn_ds_read_tr16_b64_v4i16((AS3 s16x4*)(size_t)a); }
DI s16x8 trread2A(unsigned a0, unsigned a1) { s16x4 a = trreadA(a0), b = trreadA(a1); return __builtin_shufflevector(a, b, 0, 1, 2, 3, 4, 5, 6, 7); }
DI s16x8 ldsr128A(unsigned a) { return *(const AS3 s16x8*)(size_t)a; }
DI s16x8 trread2(const char* lds, unsigned off0, unsigned off1) {
  s16x4 a = trread(lds, off0), b = trread(lds, off1);
  return __builtin_shufflevector(a, b, 0, 1, 2, 3, 4, 5, 6, 7);
}
DI s16x8 ldsr128(const char* lds, unsigned off) { return *(const s16x8*)(lds + off); }
DI void dma_d(__amdgpu_buffer_rsrc_t rs, unsigned voff, unsigned soff, unsigned ldsaddr) {
  asm volatile("s_mov_b32 m0, %0\n\ts_nop 0\n\tbuffer_load_dwordx4 %1, %2, %3 offen lds" ::"s"(ldsaddr), "v"(voff), "s"(rs), "s"(soff) : "memory");
}
DI void dma_nt(__amdgpu_buffer_rsrc_t rs, unsigned voff, unsigned soff, unsigned ldsaddr) {
  asm volatile("s_mov_b32 m0, %0\n\ts_nop 0\n\tbuffer_load_dwordx4 %1, %2, %3 offen nt lds" ::"s"(ldsaddr), "v"(voff), "s"(rs), "s"(soff) : "memory");
}
DI __amdgpu_buffer_rsrc_t mkrsrc(const void* p, unsigned bytes) {
  return __builtin_amdgcn_make_buffer_rsrc((void*)p, 0, bytes, 0x00020000);
}
DI float xmax16_32(float v) {
  unsigned u = __float_as_uint(v);
  auto r = __builtin_amdgcn_permlane16_swap(u, u, false, false);
  v = fmaxf(__uint_as_float(r[0]), __uint_as_float(r[1]));
  u = __float_as_uint(v);
  auto r2 = __builtin_amdgcn_permlane32_swap(u, u, false, false);
  return fmaxf(__uint_as_float(r2[0]), __uint_as_float(r2[1]));
}
DI float xsum16_32(float v) {
  unsigned u = __float_as_uint(v);
  auto r = __builtin_amdgcn_permlane16_swap(u, u, false, false);
  v = __uint_as_float(r[0]) + __uint_as_float(r[1]);
  u = __float_as_uint(v);
  auto r2 = __builtin_amdgcn_permlane32_swap(u, u, false, false);
  return __uint_as_float(r2[0]) + __uint_as_float(r2[1]);
}

__global__ __launch_bounds__(512, 2) void fused_gqa_kernel(Params p) {
  extern __shared__ __attribute__((aligned(16))) char lds[];
  const int tid = threadIdx.x, lane = tid & 63;
  const int w = __builtin_amdgcn_readfirstlane(tid >> 6);
  const int bid = blockIdx.x;
  const int kvh = bid & 7, b = (bid >> 3) & 15, br = bid >> 7;
  const int grp = bid >> 3;
  const int l15 = lane & 15, gq = lane >> 4;
  const int tq = (lane & 15) >> 2, tp = lane & 3;
  const int g = w >> 1, hh = w & 1;
  const unsigned ldsb = (unsigned)(uintptr_t)lds;

  STAMP(0);
  u32x4 cpf[4];
  {
    unsigned long long pc; unsigned clen;
    asm volatile("s_getpc_b64 %0\n.Lfgk_start:\n\ts_mov_b32 %1, .Lfgk_end-.Lfgk_start" : "=s"(pc), "=s"(clen));
    const char* cb = (const char*)pc;
#pragma unroll
    for (int i = 0; i < 4; ++i) {
      unsigned off = (unsigned)((w * 4 + i) * 1024 + lane * 16);
      off = off + 16 <= clen ? off : 0u;
      const char* a = cb + off;
      asm volatile("global_load_dwordx4 %0, %1, off" : "=v"(cpf[i]) : "v"(a) : "memory");
    }
  }
  const __amdgpu_buffer_rsrc_t rx = mkrsrc(p.x[br] + (size_t)b * 48 * 1024, 48 * 1024);
  const __amdgpu_buffer_rsrc_t rk = mkrsrc(p.k[br] + (size_t)(b * 8 + kvh) * 131072, 131072);
  const __amdgpu_buffer_rsrc_t rv = mkrsrc(p.v[br] + (size_t)(b * 8 + kvh) * 131072, 131072);
  const __amdgpu_buffer_rsrc_t rwq = mkrsrc(p.wq[br] + kvh * 1024, 512 * 8192 - kvh * 1024);
  const __amdgpu_buffer_rsrc_t rwo = mkrsrc(p.wo[br] + (size_t)kvh * 512 * 1024, 512 * 1024);

  const int wr = lane >> 3, wpos = lane & 7;
  const unsigned wsw0 = (unsigned)(wpos ^ (((wr >> 1) & 1) << 1)) * 16 + w * 128;

  unsigned wv0 = ((unsigned)wr << 13) + wsw0;
  auto issue_w = [&](__amdgpu_buffer_rsrc_t rs, int lgstride, int ks, unsigned stage_off) {
    const unsigned v0 = wv0, v1 = wv0 ^ 64;
#pragma unroll
    for (int i = 0; i < 4; ++i)
      dma_d(rs, (i & 1) ? v1 : v0, (unsigned)(ks * 32 + i * 8) << lgstride, ldsb + stage_off + w * 4096 + i * 1024);
  };
  const int kr = lane >> 4, kpos = lane & 15;
  unsigned kvo[2], vvo[2];
#pragma unroll
  for (int i = 0; i < 2; ++i) {
    const int row = (2 * w + i) * 4 + kr;
    kvo[i] = (unsigned)(row * 256 + ((kpos ^ (row & 15)) * 16));
    vvo[i] = (unsigned)(row * 256 + ((kpos ^ ((row & 7) << 1)) * 16));
  }
  auto issue_kv = [&](int n) {
    const unsigned dst = ldsb + RG + (unsigned)((4 + n) % 7) * UNIT + 2 * w * 1024;
    const unsigned so = (unsigned)(n >> 1) * 16384;
    if (n & 1) { dma_nt(rv, vvo[0], so, dst); dma_nt(rv, vvo[1], so, dst + 1024); }
    else       { dma_nt(rk, kvo[0], so, dst); dma_nt(rk, kvo[1], so, dst + 1024); }
  };

  const int rot = 2 * (b & 7);
  const unsigned xvo = (unsigned)((lane >> 3) * 1024 + (((lane & 7) ^ (((lane >> 3) >> 1) & 3)) * 16));
  auto issue_x = [&](int q) {
    const int kbi = q / 6, j = q % 6;
    const int kb = ((rot >> 1) + kbi) & 7;
    dma_d(rx, xvo ^ (unsigned)((j & 1) << 6), (unsigned)(j * 8 * 1024 + kb * 128), ldsb + XQ + kb * 6144 + j * 1024);
  };
  issue_x(w);
  issue_w(rwq, 13, (0 + rot) & 15, RG);
  issue_w(rwq, 13, (1 + rot) & 15, RG + 2 * UNIT);
#pragma unroll
  for (int i = 0; i < 5; ++i) issue_x(8 + w + 8 * i);
  STAMP(12);
  const unsigned epoch = __hip_atomic_load((const AS1 unsigned*)(p.ws + WS_EPOCH + grp * 128), __ATOMIC_RELAXED, __HIP_MEMORY_SCOPE_AGENT) + 1u;

  unsigned wtr[4];
  auto calc_wtr = [&](int ln) {
    const int gq_ = ln >> 4, tq_ = (ln & 15) >> 2, tp_ = ln & 3;
    const int wf = (((tq_ >> 1) & 1) << 1) | ((gq_ & 1) << 2);
#pragma unroll
    for (int c = 0; c < 4; ++c) wtr[c] = (unsigned)((8 * gq_ + tq_) * 128 + (((2 * c + (tp_ >> 1)) ^ wf) * 16) + 8 * (tp_ & 1)) + w * 4096;
  };
  calc_wtr(lane);

  struct Frag { s16x8 w[4]; s16x8 x[3]; };
  auto rd_frag1 = [&](Frag& f, unsigned stage_off, int ks) {
#pragma unroll
    for (int c = 0; c < 4; ++c) f.w[c] = trread2(lds, stage_off + wtr[c], stage_off + wtr[c] + 512);
#pragma unroll
    for (int rt = 0; rt < 3; ++rt) f.x[rt] = ldsr128(lds, XQ + (unsigned)(ks >> 1) * 6144 + (unsigned)((16 * rt + l15) * 128 + (((4 * (ks & 1) + gq) ^ ((l15 >> 1) & 7)) * 16)));
  };
  auto pin = [&](Frag& f) {
    asm volatile("s_waitcnt lgkmcnt(0)" : "+v"(f.w[0]), "+v"(f.w[1]), "+v"(f.w[2]), "+v"(f.w[3]), "+v"(f.x[0]), "+v"(f.x[1]), "+v"(f.x[2])::"memory");
  };
  f32x4 acc[4][3];
  auto mm = [&](Frag& f) {
#pragma unroll
    for (int c = 0; c < 4; ++c)
#pragma unroll
      for (int rt = 0; rt < 3; ++rt) acc[c][rt] = mfma16(f.w[c], f.x[rt], acc[c][rt]);
  };

#pragma unroll
  for (int c = 0; c < 4; ++c)
#pragma unroll
    for (int rt = 0; rt < 3; ++rt) acc[c][rt] = f32x4{0.f, 0.f, 0.f, 0.f};
  constexpr unsigned S0 = RG, S1 = RG + 2 * UNIT;
  Frag fa, fb;
  WAIT_V(10);
  asm volatile("" :: "v"(cpf[0]), "v"(cpf[1]), "v"(cpf[2]), "v"(cpf[3]));
  BAR();
  STAMP(1);
  rd_frag1(fa, S0, (0 + rot) & 15); pin(fa);
  issue_w(rwq, 13, (2 + rot) & 15, S0);
  WAIT_V(10);
  rd_frag1(fb, S1, (1 + rot) & 15);
  mm(fa);
  pin(fb);
  issue_w(rwq, 13, (3 + rot) & 15, S1);
  issue_kv(0); issue_kv(1); issue_kv(2);
  WAIT_V(10); BAR();
  rd_frag1(fa, S0, (2 + rot) & 15);
  mm(fb);
  pin(fa);
  issue_w(rwq, 13, (4 + rot) & 15, S0);
  WAIT_V(10);
  rd_frag1(fb, S1, (3 + rot) & 15);
  mm(fa);
  pin(fb);
  issue_w(rwq, 13, (5 + rot) & 15, S1);
#define CY1(acc) do {} while (0)
#pragma unroll 1
  for (int i = 4; i < 14; i += 2) {
    WAIT_V(4);
    CY1(cW);
    rd_frag1(fa, S0, (i + rot) & 15);
    mm(fb);
    pin(fa);
    CY1(cC);
    issue_w(rwq, 13, (i + 2 + rot) & 15, S0);
    CY1(cI);
    WAIT_V(4);
    CY1(cW);
    rd_frag1(fb, S1, (i + 1 + rot) & 15);
    mm(fa);
    pin(fb);
    CY1(cC);
    issue_w(rwq, 13, (i + 3 + rot) & 15, S1);
    CY1(cI);
  }
  WAIT_V(4);
  rd_frag1(fa, S0, (14 + rot) & 15);
  mm(fb);
  pin(fa);
  WAIT_V(0);
  rd_frag1(fb, S1, (15 + rot) & 15);
  mm(fa);
  pin(fb);
  mm(fb);
  BAR();
  STAMP(2);
#pragma unroll
  for (int c = 0; c < 4; ++c)
#pragma unroll
    for (int rt = 0; rt < 3; ++rt) {
      const int row = 16 * rt + l15;
      const int chunk = 8 * hh + 2 * c + (gq >> 1);
      u32x2 v = {cvtpk(acc[c][rt][0] * CEXP, acc[c][rt][1] * CEXP), cvtpk(acc[c][rt][2] * CEXP, acc[c][rt][3] * CEXP)};
      *(u32x2*)(lds + XQ + g * 12288 + row * 256 + ((chunk ^ l15) * 16) + (gq & 1) * 8) = v;
    }
  BAR();
  s16x8 qf[3][4];
#pragma unroll
  for (int rt = 0; rt < 3; ++rt)
#pragma unroll
    for (int s = 0; s < 4; ++s) qf[rt][s] = ldsr128(lds, XQ + g * 12288 + (16 * rt + l15) * 256 + (((4 * s + gq) ^ l15) * 16));
  issue_kv(3); issue_kv(4);

  wv0 = (wv0 & 8191u) + ((wv0 >> 13) << 10);
  STAMP(3);
  f32x4 o[8][3];
#pragma unroll
  for (int c = 0; c < 8; ++c)
#pragma unroll
    for (int rt = 0; rt < 3; ++rt) o[c][rt] = f32x4{0.f, 0.f, 0.f, 0.f};
  f32x4 lacc[3];
#pragma unroll
  for (int rt = 0; rt < 3; ++rt) lacc[rt] = f32x4{0.f, 0.f, 0.f, 0.f};
  const s16x8 ones = {0x3F80, 0x3F80, 0x3F80, 0x3F80, 0x3F80, 0x3F80, 0x3F80, 0x3F80};
  const unsigned koff = (unsigned)((32 * hh + l15) * 256 + ((gq ^ l15) * 16));
  const int vrow = 32 * hh + 4 * gq + tq;
  const int vsw = (vrow & 7) << 1;
  const unsigned voff = (unsigned)(vrow * 256 + (((tp >> 1) ^ vsw) * 16) + 8 * (tp & 1));

  typedef f32x4 Stile[2][3];
#define SB() __builtin_amdgcn_sched_barrier(0)
  auto max3a = [](float a, float b2, float c) { float r; asm("v_max3_f32 %0, %1, %2, %3" : "=v"(r) : "v"(a), "v"(b2), "v"(c)); return r; };
  auto max2a = [](float a, float b2) { float r; asm("v_max_f32_e32 %0, %1, %2" : "=v"(r) : "v"(a), "v"(b2)); return r; };
  auto xmax = [&](float v) {
    unsigned u = __float_as_uint(v);
    auto r = __builtin_amdgcn_permlane16_swap(u, u, false, false);
    v = max2a(__uint_as_float(r[0]), __uint_as_float(r[1]));
    u = __float_as_uint(v);
    auto r2 = __builtin_amdgcn_permlane32_swap(u, u, false, false);
    return max2a(__uint_as_float(r2[0]), __uint_as_float(r2[1]));
  };
  auto uoff = [&](int n) { return RG + (unsigned)((4 + n) % 7) * UNIT; };
  auto step_top = [&](int t) {
    if (t == 0) WAIT_V(8);
    else if (t <= 5) WAIT_V(6);
    else if (t == 6) WAIT_V(2);
    else WAIT_V(4);
    BAR();
    if (t == 0) issue_kv(7);
    else if (t <= 4) { issue_kv(2 * t + 6); issue_kv(2 * t + 7); }
    else if (t == 6) issue_w(rwo, 10, (0 + rot) & 15, S0);
    else if (t == 7) issue_w(rwo, 10, (1 + rot) & 15, S1);
  };
  s16x8 pf[3];
  f32x4 negm[3];
  auto stepAB = [&](Stile& sn, Stile& sc, unsigned ku, unsigned vu, float& lmax, auto DOQK, auto DOSM, auto CINIT) {
    constexpr bool doqk = decltype(DOQK)::value, dosm = decltype(DOSM)::value, cinit = decltype(CINIT)::value;
    s16x8 kf[8], vf[8];
    unsigned kb = ldsb + ku + koff; asm volatile("" : "+v"(kb));
    unsigned vb = ldsb + vu + voff; asm volatile("" : "+v"(vb));
    float e[24];
    if constexpr (doqk) {
      kf[0] = ldsr128A(kb);
      kf[1] = ldsr128A(kb + 4096);
      kf[2] = ldsr128A(kb ^ 64u);
    }
#pragma unroll
    for (int j = 0; j < 8; ++j) {
      if constexpr (doqk) {
        if (j + 3 < 8) kf[j + 3] = ldsr128A((kb ^ (unsigned)(((j + 3) >> 1) << 6)) + ((j + 3) & 1) * 4096);
      }
      if constexpr (dosm) {
        if (j >= 6) { const unsigned a = vb ^ (unsigned)((j - 6) << 5); vf[j - 6] = trread2A(a, a + 4096); }
#pragma unroll
        for (int u = 0; u < 3; ++u) {
          const int q = 3 * j + u, rt = q >> 3, idx = q & 7;
          e[q] = __builtin_amdgcn_exp2f(sc[idx >> 2][rt][idx & 3]);
        }
      }
      if constexpr (doqk) {
#pragma unroll
        for (int rt = 0; rt < 3; ++rt) {
          if (j < 2) sn[j & 1][rt] = mfma16(kf[j], qf[rt][j >> 1], cinit ? negm[rt] : f32x4{0.f, 0.f, 0.f, 0.f});
          else sn[j & 1][rt] = mfma16(kf[j], qf[rt][j >> 1], sn[j & 1][rt]);
        }
      }
      if constexpr (dosm) {
#pragma unroll
        for (int pr = 0; pr < 12; ++pr) {
          const int hi_el = 2 * pr + 1;
          if (hi_el / 3 == j) {
            const int rt = pr >> 2;
            unsigned pk = cvtpk(e[2 * pr], e[2 * pr + 1]);
            u32x4 t4 = __builtin_bit_cast(u32x4, pf[rt]); t4[pr & 3] = pk; pf[rt] = __builtin_bit_cast(s16x8, t4);
          }
        }
      }
      SB();
    }
    if constexpr (dosm) {
      { const unsigned a = vb ^ 64u; vf[2] = trread2A(a, a + 4096); }
      { const unsigned a = vb ^ 96u; vf[3] = trread2A(a, a + 4096); }
      asm volatile("s_nop 7" ::: "memory");
      SB();
#pragma unroll
      for (int rt = 0; rt < 3; ++rt) lacc[rt] = mfma16(ones, pf[rt], lacc[rt]);
      float t0 = 0.f;
#pragma unroll
      for (int c = 0; c < 8; ++c) {
        if (c + 4 < 8) { const unsigned a = vb ^ (unsigned)((c + 4) << 5); vf[c + 4] = trread2A(a, a + 4096); }
#pragma unroll
        for (int rt = 0; rt < 3; ++rt) o[c][rt] = mfma16(vf[c], pf[rt], o[c][rt]);
        if constexpr (doqk) {
          if (c < 6) {
            const int rt = c >> 1, kt = c & 1;
            if (c == 0) t0 = max3a(sn[0][0][0], sn[0][0][1], sn[0][0][2]);
            else t0 = max3a(t0, sn[kt][rt][0], sn[kt][rt][1]);
            if (c == 0) t0 = max2a(t0, sn[0][0][3]); else t0 = max3a(t0, sn[kt][rt][2], sn[kt][rt][3]);
          }
        }
        SB();
      }
      lmax = t0;
    }
  };
  using T_ = std::integral_constant<bool, true>; using F_ = std::integral_constant<bool, false>;
  auto decide = [&](Stile& sn, float lmax) {
    if (__builtin_expect(__any(lmax > THR), 0)) {
#pragma unroll
      for (int rt = 0; rt < 3; ++rt) {
        float a = fmaxf(fmaxf(sn[0][rt][0], sn[0][rt][1]), fmaxf(sn[0][rt][2], sn[0][rt][3]));
        float c2 = fmaxf(fmaxf(sn[1][rt][0], sn[1][rt][1]), fmaxf(sn[1][rt][2], sn[1][rt][3]));
        const float dl = fmaxf(xmax16_32(fmaxf(a, c2)), 0.f);
        const float al = __builtin_amdgcn_exp2f(-dl);
        negm[rt] -= dl; lacc[rt] *= al;
        sn[0][rt] -= dl; sn[1][rt] -= dl;
#pragma unroll
        for (int c = 0; c < 8; ++c) o[c][rt] *= al;
      }
    }
  };

  Stile sa, sb;
  float lmax;
  stepAB(sa, sb, uoff(0), 0, lmax, T_{}, F_{}, F_{});
  issue_kv(5); issue_kv(6);
#pragma unroll
  for (int rt = 0; rt < 3; ++rt) {
    float a = fmaxf(fmaxf(sa[0][rt][0], sa[0][rt][1]), fmaxf(sa[0][rt][2], sa[0][rt][3]));
    float c2 = fmaxf(fmaxf(sa[1][rt][0], sa[1][rt][1]), fmaxf(sa[1][rt][2], sa[1][rt][3]));
    const float m0 = xmax16_32(fmaxf(a, c2));
    negm[rt] = f32x4{-m0, -m0, -m0, -m0};
    sa[0][rt] -= m0; sa[1][rt] -= m0;
  }
#define CY(acc) do {} while (0)
#pragma unroll 1
  for (int t = 0; t < 6; t += 2) {
    step_top(t);
    CY(cyT);
    stepAB(sb, sa, uoff(2 * t + 2), uoff(2 * t + 1), lmax, T_{}, T_{}, T_{});
    decide(sb, lmax);
    CY(cyA);
    step_top(t + 1);
    CY(cyT);
    stepAB(sa, sb, uoff(2 * t + 4), uoff(2 * t + 3), lmax, T_{}, T_{}, T_{});
    decide(sa, lmax);
    CY(cyA);
  }
  step_top(6);
  stepAB(sb, sa, uoff(14), uoff(13), lmax, T_{}, T_{}, T_{});
  decide(sb, lmax);
  step_top(7);
  stepAB(sa, sb, 0, uoff(15), lmax, F_{}, T_{}, F_{});

  STAMP(4);
  float mrow[3], lrow[3];
#pragma unroll
  for (int rt = 0; rt < 3; ++rt) { lrow[rt] = lacc[rt][0]; mrow[rt] = -negm[rt][0]; }
  {
    int lane2 = lane; asm volatile("" : "+v"(lane2));
    const int l15b = lane2 & 15, gqb = lane2 >> 4;
    float* stat = (float*)(lds + RG + 6 * UNIT);
    if (gqb == 0) {
#pragma unroll
      for (int rt = 0; rt < 3; ++rt) { stat[((w * 3 + rt) * 16 + l15b) * 2] = mrow[rt]; stat[((w * 3 + rt) * 16 + l15b) * 2 + 1] = lrow[rt]; }
    }
    auto send = [&](f32x4 (&oo)[8][3], int base) {
#pragma unroll
      for (int cp = 0; cp < 2; ++cp)
#pragma unroll
        for (int rt = 0; rt < 3; ++rt) {
          const f32x4 a0 = oo[base + 2 * cp][rt], a1 = oo[base + 2 * cp + 1][rt];
          u32x4 pk = {cvtpk(a0[0], a0[1]), cvtpk(a0[2], a0[3]), cvtpk(a1[0], a1[1]), cvtpk(a1[2], a1[3])};
          *(u32x4*)(lds + XQ + w * 6144 + (cp * 3 + rt) * 1024 + lane2 * 16) = pk;
        }
    };
    if (hh) send(o, 0); else send(o, 4);
    BAR();
    STAMP(13);
    float s1[3], s2[3];
#pragma unroll
    for (int rt = 0; rt < 3; ++rt) {
      const float m2 = stat[(((w ^ 1) * 3 + rt) * 16 + l15b) * 2], l2 = stat[(((w ^ 1) * 3 + rt) * 16 + l15b) * 2 + 1];
      const float mn = fmaxf(mrow[rt], m2);
      const float w1 = __builtin_amdgcn_exp2f(mrow[rt] - mn), w2 = __builtin_amdgcn_exp2f(m2 - mn);
      const float inv = __builtin_amdgcn_rcpf(lrow[rt] * w1 + l2 * w2);
      s1[rt] = w1 * inv; s2[rt] = w2 * inv;
    }
    auto recv = [&](f32x4 (&oo)[8][3], int base) {
      u32x4 r[6];
#pragma unroll
      for (int i = 0; i < 6; ++i) r[i] = *(const u32x4*)(lds + XQ + (w ^ 1) * 6144 + i * 1024 + lane2 * 16);
      asm volatile("s_waitcnt lgkmcnt(0)" : "+v"(r[0]), "+v"(r[1]), "+v"(r[2]), "+v"(r[3]), "+v"(r[4]), "+v"(r[5]) :: "memory");
#pragma unroll
      for (int cp = 0; cp < 2; ++cp)
#pragma unroll
        for (int rt = 0; rt < 3; ++rt) {
#pragma unroll
          for (int h2 = 0; h2 < 2; ++h2) {
            const f32x4 a = oo[base + 2 * cp + h2][rt];
            const u32x4 rr = r[cp * 3 + rt];
            const float v0 = a[0] * s1[rt] + __uint_as_float(rr[2 * h2] << 16) * s2[rt];
            const float v1 = a[1] * s1[rt] + __uint_as_float(rr[2 * h2] & 0xffff0000u) * s2[rt];
            const float v2 = a[2] * s1[rt] + __uint_as_float(rr[2 * h2 + 1] << 16) * s2[rt];
            const float v3 = a[3] * s1[rt] + __uint_as_float(rr[2 * h2 + 1] & 0xffff0000u) * s2[rt];
            const int row = 16 * rt + l15b;
            const int chunk = 2 * (2 * cp + h2) + (gqb >> 1);
            u32x2 pk = {cvtpk(v0, v1), cvtpk(v2, v3)};
            *(u32x2*)(lds + XQ + (w ^ 1) * 6144 + row * 128 + ((chunk ^ ((l15b >> 1) & 7)) * 16) + (gqb & 1) * 8) = pk;
          }
        }
    };
    STAMP(14);
    if (hh) recv(o, 4); else recv(o, 0);
    BAR();
  }

  STAMP(5);
  int lane3 = lane; asm volatile("" : "+v"(lane3));
  calc_wtr(lane3);
  const int l15c = lane3 & 15, gqc = lane3 >> 4;
  auto rd_frag3 = [&](Frag& f, unsigned stage_off, int ks) {
#pragma unroll
    for (int c = 0; c < 4; ++c) f.w[c] = trread2(lds, stage_off + wtr[c], stage_off + wtr[c] + 512);
    const unsigned blk = XQ + (unsigned)((ks >> 1) ^ 1) * 6144;
#pragma unroll
    for (int rt = 0; rt < 3; ++rt) f.x[rt] = ldsr128(lds, blk + (unsigned)((16 * rt + l15c) * 128 + (((4 * (ks & 1) + gqc) ^ ((l15c >> 1) & 7)) * 16)));
  };
#pragma unroll
  for (int c = 0; c < 4; ++c)
#pragma unroll
    for (int rt = 0; rt < 3; ++rt) acc[c][rt] = f32x4{0.f, 0.f, 0.f, 0.f};
  WAIT_V(4);
  rd_frag3(fa, S0, (0 + rot) & 15); pin(fa);
  issue_w(rwo, 10, (2 + rot) & 15, S0);
  WAIT_V(4);
  rd_frag3(fb, S1, (1 + rot) & 15);
  mm(fa);
  pin(fb);
  issue_w(rwo, 10, (3 + rot) & 15, S1);
#define CY3(acc) do {} while (0)
#pragma unroll 1
  for (int i = 2; i < 14; i += 2) {
    WAIT_V(4);
    CY3(cW);
    rd_frag3(fa, S0, (i + rot) & 15);
    mm(fb);
    pin(fa);
    CY3(cC);
    issue_w(rwo, 10, (i + 2 + rot) & 15, S0);
    CY3(cI);
    WAIT_V(4);
    CY3(cW);
    rd_frag3(fb, S1, (i + 1 + rot) & 15);
    mm(fa);
    pin(fb);
    CY3(cC);
    issue_w(rwo, 10, (i + 3 + rot) & 15, S1);
    CY3(cI);
  }
  WAIT_V(4);
  rd_frag3(fa, S0, (14 + rot) & 15);
  mm(fb);
  pin(fa);
  WAIT_V(0);
  rd_frag3(fb, S1, (15 + rot) & 15);
  mm(fa);
  pin(fb);
  mm(fb);

  STAMP(6);
  {
    char* part = p.ws + WS_PART;
    const __amdgpu_buffer_rsrc_t rs = __builtin_amdgcn_make_buffer_rsrc(part + (size_t)grp * 8 * 8 * 6144, 0, 8 * 8 * 6144, 0x00020000);
    const unsigned base = (unsigned)((w * 8 + kvh) * 6144);
#pragma unroll
    for (int cp = 0; cp < 2; ++cp)
#pragma unroll
      for (int rt = 0; rt < 3; ++rt) {
        const f32x4 a0 = acc[2 * cp][rt], a1 = acc[2 * cp + 1][rt];
        auto r0 = __builtin_amdgcn_permlane16_swap(cvtpk(a0[0], a0[1]), cvtpk(a1[0], a1[1]), false, false);
        auto r1 = __builtin_amdgcn_permlane16_swap(cvtpk(a0[2], a0[3]), cvtpk(a1[2], a1[3]), false, false);
        u32x4 pk = {r0[0], r1[0], r0[1], r1[1]};
        const unsigned off = base + (unsigned)((16 * rt + l15c) * 128 + (16 * (2 * cp + (gqc & 1)) + 8 * (gqc >> 1)) * 2);
        __builtin_amdgcn_raw_buffer_store_b128(pk, rs, off, 0, 16);
      }
    WAIT_V(0);
    STAMP(7);
    AS1 unsigned* flags = (AS1 unsigned*)(p.ws + WS_FLAG + (size_t)grp * 64 * 64);
    if (lane == 0) __hip_atomic_store(flags + (w * 8 + kvh) * 16, epoch, __ATOMIC_RELAXED, __HIP_MEMORY_SCOPE_AGENT);
    for (unsigned spins = 0; spins < 4000000u; ++spins) {
      unsigned f = epoch;
      if (lane < 8) f = __hip_atomic_load(flags + (kvh * 8 + lane) * 16, __ATOMIC_RELAXED, __HIP_MEMORY_SCOPE_AGENT);
      if (__all(f == epoch)) break;
      __builtin_amdgcn_s_sleep(1);
    }
    asm volatile("" ::: "memory");
    STAMP(8);
    if (tid == 0) __hip_atomic_store((AS1 unsigned*)(p.ws + WS_EPOCH + grp * 128), epoch, __ATOMIC_RELAXED, __HIP_MEMORY_SCOPE_AGENT);
    char* outp = p.out + (size_t)b * 48 * 2048 + br * 1024 + kvh * 128;
    const int q8 = w * 48 + lane;
    if (lane < 48) {
      u32x4 v[8];
#pragma unroll
      for (int sidx = 0; sidx < 8; ++sidx) v[sidx] = __builtin_amdgcn_raw_buffer_load_b128(rs, (unsigned)((kvh * 8 + sidx) * 6144 + q8 * 16), 0, 16);
      float s[8];
#pragma unroll
      for (int j = 0; j < 8; ++j) s[j] = 0.f;
#pragma unroll
      for (int sidx = 0; sidx < 8; ++sidx)
#pragma unroll
        for (int j = 0; j < 4; ++j) { s[2 * j] += __uint_as_float(v[sidx][j] << 16); s[2 * j + 1] += __uint_as_float(v[sidx][j] & 0xffff0000u); }
      const int row = q8 >> 3, c8 = q8 & 7;
      u32x4 pk = {cvtpk(s[0], s[1]), cvtpk(s[2], s[3]), cvtpk(s[4], s[5]), cvtpk(s[6], s[7])};
      *(u32x4*)(outp + (size_t)row * 2048 + c8 * 16) = pk;
    }
  }
  STAMP(9);
  asm volatile(".Lfgk_end:");
}

extern "C" void kernel_launch(void* const* d_in, const int* in_sizes, int n_in, void* d_out, int out_size, void* d_ws, size_t ws_size,
                              hipStream_t stream) {
  Params p;
  for (int br = 0; br < 2; ++br) {
    p.x[br] = (const char*)d_in[5 * br + 0];
    p.k[br] = (const char*)d_in[5 * br + 1];
    p.v[br] = (const char*)d_in[5 * br + 2];
    p.wq[br] = (const char*)d_in[5 * br + 3];
    p.wo[br] = (const char*)d_in[5 * br + 4];
  }
  p.out = (char*)d_out;
  p.ws = (char*)d_ws;
  (void)hipFuncSetAttribute((const void*)fused_gqa_kernel, hipFuncAttributeMaxDynamicSharedMemorySize, LDS_BYTES);
  fused_gqa_kernel<<<256, 512, LDS_BYTES, stream>>>(p);
}
```

```cpp
#include <hip/hip_runtime.h>
#include <hip/hip_bf16.h>
#include <stdint.h>
#include <type_traits>

typedef short s16x4 __attribute__((ext_vector_type(4)));
typedef short s16x8 __attribute__((ext_vector_type(8)));
typedef __bf16 bf16x8 __attribute__((ext_vector_type(8)));
typedef __bf16 bf16x2 __attribute__((ext_vector_type(2)));
typedef float f32x4 __attribute__((ext_vector_type(4)));
typedef float f32x2 __attribute__((ext_vector_type(2)));
typedef unsigned u32x4 __attribute__((ext_vector_type(4)));
typedef unsigned u32x2 __attribute__((ext_vector_type(2)));

#define AS1 __attribute__((address_space(1)))
#define AS3 __attribute__((address_space(3)))
#define DI __device__ __forceinline__

struct Params {
  const char* x[2];
  const char* k[2];
  const char* v[2];
  const char* wq[2];
  const char* wo[2];
  char* out;
  char* ws;
};

constexpr unsigned XQ = 0;
constexpr unsigned RG = 49152;
constexpr unsigned UNIT = 16384;
constexpr int LDS_BYTES = 163840;

constexpr size_t WS_EPOCH = 0;
constexpr size_t WS_FLAG = 8192;
constexpr size_t WS_PART = 262144;

constexpr float CEXP = 0.08838834764831845f * 1.4426950408889634f;
constexpr float THR = 8.0f;

#define WAIT_V(n) asm volatile("s_waitcnt vmcnt(" #n ")" ::: "memory")
#define BAR() do { asm volatile("s_waitcnt lgkmcnt(0)" ::: "memory"); __builtin_amdgcn_s_barrier(); asm volatile("" ::: "memory"); } while (0)
#define STAMP(i) do {} while (0)

DI f32x4 mfma16(s16x8 a, s16x8 b, f32x4 c) {
  return __builtin_amdgcn_mfma_f32_16x16x32_bf16(__builtin_bit_cast(bf16x8, a), __builtin_bit_cast(bf16x8, b), c, 0, 0, 0);
}
DI unsigned cvtpk(float lo, float hi) {
  f32x2 v = {lo, hi};
  bf16x2 b = __builtin_convertvector(v, bf16x2);
  return __builtin_bit_cast(unsigned, b);
}
DI s16x4 trread(const char* lds, unsigned off) {
  return __builtin_amdgcn_ds_read_tr16_b64_v4i16((AS3 s16x4*)(lds + off));
}
DI s16x4 trreadA(unsigned a) { return __builtin_amdgcn_ds_read_tr16_b64_v4i16((AS3 s16x4*)(size_t)a); }
DI s16x8 trread2A(unsigned a0, unsigned a1) { s16x4 a = trreadA(a0), b = trreadA(a1); return __builtin_shufflevector(a, b, 0, 1, 2, 3, 4, 5, 6, 7); }
DI s16x8 ldsr128A(unsigned a) { return *(const AS3 s16x8*)(size_t)a; }
DI s16x8 trread2(const char* lds, unsigned off0, unsigned off1) {
  s16x4 a = trread(lds, off0), b = trread(lds, off1);
  return __builtin_shufflevector(a, b, 0, 1, 2, 3, 4, 5, 6, 7);
}
DI s16x8 ldsr128(const char* lds, unsigned off) { return *(const s16x8*)(lds + off); }
DI void dma_d(__amdgpu_buffer_rsrc_t rs, unsigned voff, unsigned soff, unsigned ldsaddr) {
  asm volatile("s_mov_b32 m0, %0\n\ts_nop 0\n\tbuffer_load_dwordx4 %1, %2, %3 offen lds" ::"s"(ldsaddr), "v"(voff), "s"(rs), "s"(soff) : "memory");
}
DI void dma_nt(__amdgpu_buffer_rsrc_t rs, unsigned voff, unsigned soff, unsigned ldsaddr) {
  asm volatile("s_mov_b32 m0, %0\n\ts_nop 0\n\tbuffer_load_dwordx4 %1, %2, %3 offen nt lds" ::"s"(ldsaddr), "v"(voff), "s"(rs), "s"(soff) : "memory");
}
DI __amdgpu_buffer_rsrc_t mkrsrc(const void* p, unsigned bytes) {
  return __builtin_amdgcn_make_buffer_rsrc((void*)p, 0, bytes, 0x00020000);
}
DI float xmax16_32(float v) {
  unsigned u = __float_as_uint(v);
  auto r = __builtin_amdgcn_permlane16_swap(u, u, false, false);
  v = fmaxf(__uint_as_float(r[0]), __uint_as_float(r[1]));
  u = __float_as_uint(v);
  auto r2 = __builtin_amdgcn_permlane32_swap(u, u, false, false);
  return fmaxf(__uint_as_float(r2[0]), __uint_as_float(r2[1]));
}
DI float xsum16_32(float v) {
  unsigned u = __float_as_uint(v);
  auto r = __builtin_amdgcn_permlane16_swap(u, u, false, false);
  v = __uint_as_float(r[0]) + __uint_as_float(r[1]);
  u = __float_as_uint(v);
  auto r2 = __builtin_amdgcn_permlane32_swap(u, u, false, false);
  return __uint_as_float(r2[0]) + __uint_as_float(r2[1]);
}

__global__ __launch_bounds__(512, 2) void fused_gqa_kernel(Params p) {
  extern __shared__ __attribute__((aligned(16))) char lds[];
  const int tid = threadIdx.x, lane = tid & 63;
  const int w = __builtin_amdgcn_readfirstlane(tid >> 6);
  const int bid = blockIdx.x;
  const int kvh = bid & 7, b = (bid >> 3) & 15, br = bid >> 7;
  const int grp = bid >> 3;
  const int l15 = lane & 15, gq = lane >> 4;
  const int tq = (lane & 15) >> 2, tp = lane & 3;
  const int g = w >> 1, hh = w & 1;
  const unsigned ldsb = (unsigned)(uintptr_t)lds;

  STAMP(0);
  asm volatile("" :: "s"(p.ws), "s"(p.out), "s"(p.x[br]), "s"(p.k[br]), "s"(p.v[br]), "s"(p.wq[br]), "s"(p.wo[br]));
  const __amdgpu_buffer_rsrc_t rx = mkrsrc(p.x[br] + (size_t)b * 48 * 1024, 48 * 1024);
  const __amdgpu_buffer_rsrc_t rk = mkrsrc(p.k[br] + (size_t)(b * 8 + kvh) * 131072, 131072);
  const __amdgpu_buffer_rsrc_t rv = mkrsrc(p.v[br] + (size_t)(b * 8 + kvh) * 131072, 131072);
  const __amdgpu_buffer_rsrc_t rwq = mkrsrc(p.wq[br] + kvh * 1024, 512 * 8192 - kvh * 1024);
  const __amdgpu_buffer_rsrc_t rwo = mkrsrc(p.wo[br] + (size_t)kvh * 512 * 1024, 512 * 1024);

  const int wr = lane >> 3, wpos = lane & 7;
  const unsigned wsw0 = (unsigned)(wpos ^ (((wr >> 1) & 1) << 1)) * 16 + w * 128;

  unsigned wv0 = ((unsigned)wr << 13) + wsw0;
  auto issue_w = [&](__amdgpu_buffer_rsrc_t rs, int lgstride, int ks, unsigned stage_off) {
    const unsigned v0 = wv0, v1 = wv0 ^ 64;
#pragma unroll
    for (int i = 0; i < 4; ++i)
      dma_d(rs, (i & 1) ? v1 : v0, (unsigned)(ks * 32 + i * 8) << lgstride, ldsb + stage_off + w * 4096 + i * 1024);
  };
  const int kr = lane >> 4, kpos = lane & 15;
  unsigned kvo[2], vvo[2];
#pragma unroll
  for (int i = 0; i < 2; ++i) {
    const int row = (2 * w + i) * 4 + kr;
    kvo[i] = (unsigned)(row * 256 + ((kpos ^ (row & 15)) * 16));
    vvo[i] = (unsigned)(row * 256 + ((kpos ^ ((row & 7) << 1)) * 16));
  }
  auto issue_kv = [&](int n) {
    const unsigned dst = ldsb + RG + (unsigned)((4 + n) % 7) * UNIT + 2 * w * 1024;
    const unsigned so = (unsigned)(n >> 1) * 16384;
    if (n & 1) { dma_nt(rv, vvo[0], so, dst); dma_nt(rv, vvo[1], so, dst + 1024); }
    else       { dma_nt(rk, kvo[0], so, dst); dma_nt(rk, kvo[1], so, dst + 1024); }
  };

  const int rot = 2 * (b & 7);
  const unsigned xvo = (unsigned)((lane >> 3) * 1024 + (((lane & 7) ^ (((lane >> 3) >> 1) & 3)) * 16));
  auto issue_x = [&](int q) {
    const int kbi = q / 6, j = q % 6;
    const int kb = ((rot >> 1) + kbi) & 7;
    dma_d(rx, xvo ^ (unsigned)((j & 1) << 6), (unsigned)(j * 8 * 1024 + kb * 128), ldsb + XQ + kb * 6144 + j * 1024);
  };
  issue_x(w);
  issue_w(rwq, 13, (0 + rot) & 15, RG);
  issue_w(rwq, 13, (1 + rot) & 15, RG + 2 * UNIT);
#pragma unroll
  for (int i = 0; i < 5; ++i) issue_x(8 + w + 8 * i);
  STAMP(12);
  const unsigned epoch = __hip_atomic_load((const AS1 unsigned*)(p.ws + WS_EPOCH + grp * 128), __ATOMIC_RELAXED, __HIP_MEMORY_SCOPE_AGENT) + 1u;

  unsigned wtr[4];
  auto calc_wtr = [&](int ln) {
    const int gq_ = ln >> 4, tq_ = (ln & 15) >> 2, tp_ = ln & 3;
    const int wf = (((tq_ >> 1) & 1) << 1) | ((gq_ & 1) << 2);
#pragma unroll
    for (int c = 0; c < 4; ++c) wtr[c] = (unsigned)((8 * gq_ + tq_) * 128 + (((2 * c + (tp_ >> 1)) ^ wf) * 16) + 8 * (tp_ & 1)) + w * 4096;
  };
  calc_wtr(lane);

  struct Frag { s16x8 w[4]; s16x8 x[3]; };
  auto rd_frag1 = [&](Frag& f, unsigned stage_off, int ks) {
#pragma unroll
    for (int c = 0; c < 4; ++c) f.w[c] = trread2(lds, stage_off + wtr[c], stage_off + wtr[c] + 512);
#pragma unroll
    for (int rt = 0; rt < 3; ++rt) f.x[rt] = ldsr128(lds, XQ + (unsigned)(ks >> 1) * 6144 + (unsigned)((16 * rt + l15) * 128 + (((4 * (ks & 1) + gq) ^ ((l15 >> 1) & 7)) * 16)));
  };
  auto pin = [&](Frag& f) {
    asm volatile("s_waitcnt lgkmcnt(0)" : "+v"(f.w[0]), "+v"(f.w[1]), "+v"(f.w[2]), "+v"(f.w[3]), "+v"(f.x[0]), "+v"(f.x[1]), "+v"(f.x[2])::"memory");
  };
  f32x4 acc[4][3];
  auto mm = [&](Frag& f) {
#pragma unroll
    for (int c = 0; c < 4; ++c)
#pragma unroll
      for (int rt = 0; rt < 3; ++rt) acc[c][rt] = mfma16(f.w[c], f.x[rt], acc[c][rt]);
  };

#pragma unroll
  for (int c = 0; c < 4; ++c)
#pragma unroll
    for (int rt = 0; rt < 3; ++rt) acc[c][rt] = f32x4{0.f, 0.f, 0.f, 0.f};
  constexpr unsigned S0 = RG, S1 = RG + 2 * UNIT;
  Frag fa, fb;
  WAIT_V(10); BAR();
  STAMP(1);
  rd_frag1(fa, S0, (0 + rot) & 15); pin(fa);
  issue_w(rwq, 13, (2 + rot) & 15, S0);
  WAIT_V(10);
  rd_frag1(fb, S1, (1 + rot) & 15);
  mm(fa);
  pin(fb);
  issue_w(rwq, 13, (3 + rot) & 15, S1);
  WAIT_V(4); BAR();
  rd_frag1(fa, S0, (2 + rot) & 15);
  mm(fb);
  pin(fa);
  issue_w(rwq, 13, (4 + rot) & 15, S0);
  WAIT_V(4);
  rd_frag1(fb, S1, (3 + rot) & 15);
  mm(fa);
  pin(fb);
  issue_w(rwq, 13, (5 + rot) & 15, S1);
#define CY1(acc) do {} while (0)
#pragma unroll 1
  for (int i = 4; i < 14; i += 2) {
    WAIT_V(4);
    CY1(cW);
    rd_frag1(fa, S0, (i + rot) & 15);
    mm(fb);
    pin(fa);
    CY1(cC);
    issue_w(rwq, 13, (i + 2 + rot) & 15, S0);
    CY1(cI);
    WAIT_V(4);
    CY1(cW);
    rd_frag1(fb, S1, (i + 1 + rot) & 15);
    mm(fa);
    pin(fb);
    CY1(cC);
    issue_w(rwq, 13, (i + 3 + rot) & 15, S1);
    CY1(cI);
  }
  issue_kv(0); issue_kv(1); issue_kv(2);
  WAIT_V(10);
  rd_frag1(fa, S0, (14 + rot) & 15);
  mm(fb);
  pin(fa);
  WAIT_V(6);
  rd_frag1(fb, S1, (15 + rot) & 15);
  mm(fa);
  pin(fb);
  mm(fb);
  BAR();
  STAMP(2);
#pragma unroll
  for (int c = 0; c < 4; ++c)
#pragma unroll
    for (int rt = 0; rt < 3; ++rt) {
      const int row = 16 * rt + l15;
      const int chunk = 8 * hh + 2 * c + (gq >> 1);
      u32x2 v = {cvtpk(acc[c][rt][0] * CEXP, acc[c][rt][1] * CEXP), cvtpk(acc[c][rt][2] * CEXP, acc[c][rt][3] * CEXP)};
      *(u32x2*)(lds + XQ + g * 12288 + row * 256 + ((chunk ^ l15) * 16) + (gq & 1) * 8) = v;
    }
  WAIT_V(4);
  BAR();
  s16x8 qf[3][4];
#pragma unroll
  for (int rt = 0; rt < 3; ++rt)
#pragma unroll
    for (int s = 0; s < 4; ++s) qf[rt][s] = ldsr128(lds, XQ + g * 12288 + (16 * rt + l15) * 256 + (((4 * s + gq) ^ l15) * 16));
  issue_kv(3); issue_kv(4);

  wv0 = (wv0 & 8191u) + ((wv0 >> 13) << 10);
  STAMP(3);
  f32x4 o[8][3];
#pragma unroll
  for (int c = 0; c < 8; ++c)
#pragma unroll
    for (int rt = 0; rt < 3; ++rt) o[c][rt] = f32x4{0.f, 0.f, 0.f, 0.f};
  f32x4 lacc[3];
#pragma unroll
  for (int rt = 0; rt < 3; ++rt) lacc[rt] = f32x4{0.f, 0.f, 0.f, 0.f};
  const s16x8 ones = {0x3F80, 0x3F80, 0x3F80, 0x3F80, 0x3F80, 0x3F80, 0x3F80, 0x3F80};
  const unsigned koff = (unsigned)((32 * hh + l15) * 256 + ((gq ^ l15) * 16));
  const int vrow = 32 * hh + 4 * gq + tq;
  const int vsw = (vrow & 7) << 1;
  const unsigned voff = (unsigned)(vrow * 256 + (((tp >> 1) ^ vsw) * 16) + 8 * (tp & 1));

  typedef f32x4 Stile[2][3];
#define SB() __builtin_amdgcn_sched_barrier(0)
  auto max3a = [](float a, float b2, float c) { float r; asm("v_max3_f32 %0, %1, %2, %3" : "=v"(r) : "v"(a), "v"(b2), "v"(c)); return r; };
  auto max2a = [](float a, float b2) { float r; asm("v_max_f32_e32 %0, %1, %2" : "=v"(r) : "v"(a), "v"(b2)); return r; };
  auto xmax = [&](float v) {
    unsigned u = __float_as_uint(v);
    auto r = __builtin_amdgcn_permlane16_swap(u, u, false, false);
    v = max2a(__uint_as_float(r[0]), __uint_as_float(r[1]));
    u = __float_as_uint(v);
    auto r2 = __builtin_amdgcn_permlane32_swap(u, u, false, false);
    return max2a(__uint_as_float(r2[0]), __uint_as_float(r2[1]));
  };
  auto uoff = [&](int n) { return RG + (unsigned)((4 + n) % 7) * UNIT; };
  auto step_top = [&](int t) {
    if (t == 0) WAIT_V(8);
    else if (t <= 5) WAIT_V(6);
    else if (t == 6) WAIT_V(2);
    else WAIT_V(4);
    BAR();
    if (t == 0) issue_kv(7);
    else if (t <= 4) { issue_kv(2 * t + 6); issue_kv(2 * t + 7); }
    else if (t == 6) issue_w(rwo, 10, (0 + rot) & 15, S0);
    else if (t == 7) issue_w(rwo, 10, (1 + rot) & 15, S1);
  };
  s16x8 pf[3];
  f32x4 negm[3];
  auto stepAB = [&](Stile& sn, Stile& sc, unsigned ku, unsigned vu, float& lmax, auto DOQK, auto DOSM, auto CINIT) {
    constexpr bool doqk = decltype(DOQK)::value, dosm = decltype(DOSM)::value, cinit = decltype(CINIT)::value;
    s16x8 kf[8], vf[8];
    unsigned kb = ldsb + ku + koff; asm volatile("" : "+v"(kb));
    unsigned vb = ldsb + vu + voff; asm volatile("" : "+v"(vb));
    float e[24];
    if constexpr (doqk) {
      kf[0] = ldsr128A(kb);
      kf[1] = ldsr128A(kb + 4096);
      kf[2] = ldsr128A(kb ^ 64u);
    }
#pragma unroll
    for (int j = 0; j < 8; ++j) {
      if constexpr (doqk) {
        if (j + 3 < 8) kf[j + 3] = ldsr128A((kb ^ (unsigned)(((j + 3) >> 1) << 6)) + ((j + 3) & 1) * 4096);
      }
      if constexpr (dosm) {
        if (j >= 6) { const unsigned a = vb ^ (unsigned)((j - 6) << 5); vf[j - 6] = trread2A(a, a + 4096); }
#pragma unroll
        for (int u = 0; u < 3; ++u) {
          const int q = 3 * j + u, rt = q >> 3, idx = q & 7;
          e[q] = __builtin_amdgcn_exp2f(sc[idx >> 2][rt][idx & 3]);
        }
      }
      if constexpr (doqk) {
#pragma unroll
        for (int rt = 0; rt < 3; ++rt) {
          if (j < 2) sn[j & 1][rt] = mfma16(kf[j], qf[rt][j >> 1], cinit ? negm[rt] : f32x4{0.f, 0.f, 0.f, 0.f});
          else sn[j & 1][rt] = mfma16(kf[j], qf[rt][j >> 1], sn[j & 1][rt]);
        }
      }
      if constexpr (dosm) {
#pragma unroll
        for (int pr = 0; pr < 12; ++pr) {
          const int hi_el = 2 * pr + 1;
          if (hi_el / 3 == j) {
            const int rt = pr >> 2;
            unsigned pk = cvtpk(e[2 * pr], e[2 * pr + 1]);
            u32x4 t4 = __builtin_bit_cast(u32x4, pf[rt]); t4[pr & 3] = pk; pf[rt] = __builtin_bit_cast(s16x8, t4);
          }
        }
      }
      SB();
    }
    if constexpr (dosm) {
      { const unsigned a = vb ^ 64u; vf[2] = trread2A(a, a + 4096); }
      { const unsigned a = vb ^ 96u; vf[3] = trread2A(a, a + 4096); }
      asm volatile("s_nop 7" ::: "memory");
      SB();
#pragma unroll
      for (int rt = 0; rt < 3; ++rt) lacc[rt] = mfma16(ones, pf[rt], lacc[rt]);
      float t0 = 0.f;
#pragma unroll
      for (int c = 0; c < 8; ++c) {
        if (c + 4 < 8) { const unsigned a = vb ^ (unsigned)((c + 4) << 5); vf[c + 4] = trread2A(a, a + 4096); }
#pragma unroll
        for (int rt = 0; rt < 3; ++rt) o[c][rt] = mfma16(vf[c], pf[rt], o[c][rt]);
        if constexpr (doqk) {
          if (c < 6) {
            const int rt = c >> 1, kt = c & 1;
            if (c == 0) t0 = max3a(sn[0][0][0], sn[0][0][1], sn[0][0][2]);
            else t0 = max3a(t0, sn[kt][rt][0], sn[kt][rt][1]);
            if (c == 0) t0 = max2a(t0, sn[0][0][3]); else t0 = max3a(t0, sn[kt][rt][2], sn[kt][rt][3]);
          }
        }
        SB();
      }
      lmax = t0;
    }
  };
  using T_ = std::integral_constant<bool, true>; using F_ = std::integral_constant<bool, false>;
  auto decide = [&](Stile& sn, float lmax) {
    if (__builtin_expect(__any(lmax > THR), 0)) {
#pragma unroll
      for (int rt = 0; rt < 3; ++rt) {
        float a = fmaxf(fmaxf(sn[0][rt][0], sn[0][rt][1]), fmaxf(sn[0][rt][2], sn[0][rt][3]));
        float c2 = fmaxf(fmaxf(sn[1][rt][0], sn[1][rt][1]), fmaxf(sn[1][rt][2], sn[1][rt][3]));
        const float dl = fmaxf(xmax16_32(fmaxf(a, c2)), 0.f);
        const float al = __builtin_amdgcn_exp2f(-dl);
        negm[rt] -= dl; lacc[rt] *= al;
        sn[0][rt] -= dl; sn[1][rt] -= dl;
#pragma unroll
        for (int c = 0; c < 8; ++c) o[c][rt] *= al;
      }
    }
  };

  Stile sa, sb;
  float lmax;
  stepAB(sa, sb, uoff(0), 0, lmax, T_{}, F_{}, F_{});
  issue_kv(5); issue_kv(6);
#pragma unroll
  for (int rt = 0; rt < 3; ++rt) {
    float a = fmaxf(fmaxf(sa[0][rt][0], sa[0][rt][1]), fmaxf(sa[0][rt][2], sa[0][rt][3]));
    float c2 = fmaxf(fmaxf(sa[1][rt][0], sa[1][rt][1]), fmaxf(sa[1][rt][2], sa[1][rt][3]));
    const float m0 = xmax16_32(fmaxf(a, c2));
    negm[rt] = f32x4{-m0, -m0, -m0, -m0};
    sa[0][rt] -= m0; sa[1][rt] -= m0;
  }
#define CY(acc) do {} while (0)
#pragma unroll 1
  for (int t = 0; t < 6; t += 2) {
    step_top(t);
    CY(cyT);
    stepAB(sb, sa, uoff(2 * t + 2), uoff(2 * t + 1), lmax, T_{}, T_{}, T_{});
    decide(sb, lmax);
    CY(cyA);
    step_top(t + 1);
    CY(cyT);
    stepAB(sa, sb, uoff(2 * t + 4), uoff(2 * t + 3), lmax, T_{}, T_{}, T_{});
    decide(sa, lmax);
    CY(cyA);
  }
  step_top(6);
  stepAB(sb, sa, uoff(14), uoff(13), lmax, T_{}, T_{}, T_{});
  decide(sb, lmax);
  step_top(7);
  stepAB(sa, sb, 0, uoff(15), lmax, F_{}, T_{}, F_{});

  STAMP(4);
  float mrow[3], lrow[3];
#pragma unroll
  for (int rt = 0; rt < 3; ++rt) { lrow[rt] = lacc[rt][0]; mrow[rt] = -negm[rt][0]; }
  {
    int lane2 = lane; asm volatile("" : "+v"(lane2));
    const int l15b = lane2 & 15, gqb = lane2 >> 4;
    float* stat = (float*)(lds + RG + 6 * UNIT);
    if (gqb == 0) {
#pragma unroll
      for (int rt = 0; rt < 3; ++rt) { stat[((w * 3 + rt) * 16 + l15b) * 2] = mrow[rt]; stat[((w * 3 + rt) * 16 + l15b) * 2 + 1] = lrow[rt]; }
    }
    auto send = [&](f32x4 (&oo)[8][3], int base) {
#pragma unroll
      for (int cp = 0; cp < 2; ++cp)
#pragma unroll
        for (int rt = 0; rt < 3; ++rt) {
          const f32x4 a0 = oo[base + 2 * cp][rt], a1 = oo[base + 2 * cp + 1][rt];
          u32x4 pk = {cvtpk(a0[0], a0[1]), cvtpk(a0[2], a0[3]), cvtpk(a1[0], a1[1]), cvtpk(a1[2], a1[3])};
          *(u32x4*)(lds + XQ + w * 6144 + (cp * 3 + rt) * 1024 + lane2 * 16) = pk;
        }
    };
    if (hh) send(o, 0); else send(o, 4);
    BAR();
    STAMP(13);
    float s1[3], s2[3];
#pragma unroll
    for (int rt = 0; rt < 3; ++rt) {
      const float m2 = stat[(((w ^ 1) * 3 + rt) * 16 + l15b) * 2], l2 = stat[(((w ^ 1) * 3 + rt) * 16 + l15b) * 2 + 1];
      const float mn = fmaxf(mrow[rt], m2);
      const float w1 = __builtin_amdgcn_exp2f(mrow[rt] - mn), w2 = __builtin_amdgcn_exp2f(m2 - mn);
      const float inv = __builtin_amdgcn_rcpf(lrow[rt] * w1 + l2 * w2);
      s1[rt] = w1 * inv; s2[rt] = w2 * inv;
    }
    auto recv = [&](f32x4 (&oo)[8][3], int base) {
      u32x4 r[6];
#pragma unroll
      for (int i = 0; i < 6; ++i) r[i] = *(const u32x4*)(lds + XQ + (w ^ 1) * 6144 + i * 1024 + lane2 * 16);
      asm volatile("s_waitcnt lgkmcnt(0)" : "+v"(r[0]), "+v"(r[1]), "+v"(r[2]), "+v"(r[3]), "+v"(r[4]), "+v"(r[5]) :: "memory");
#pragma unroll
      for (int cp = 0; cp < 2; ++cp)
#pragma unroll
        for (int rt = 0; rt < 3; ++rt) {
#pragma unroll
          for (int h2 = 0; h2 < 2; ++h2) {
            const f32x4 a = oo[base + 2 * cp + h2][rt];
            const u32x4 rr = r[cp * 3 + rt];
            const float v0 = a[0] * s1[rt] + __uint_as_float(rr[2 * h2] << 16) * s2[rt];
            const float v1 = a[1] * s1[rt] + __uint_as_float(rr[2 * h2] & 0xffff0000u) * s2[rt];
            const float v2 = a[2] * s1[rt] + __uint_as_float(rr[2 * h2 + 1] << 16) * s2[rt];
            const float v3 = a[3] * s1[rt] + __uint_as_float(rr[2 * h2 + 1] & 0xffff0000u) * s2[rt];
            const int row = 16 * rt + l15b;
            const int chunk = 2 * (2 * cp + h2) + (gqb >> 1);
            u32x2 pk = {cvtpk(v0, v1), cvtpk(v2, v3)};
            *(u32x2*)(lds + XQ + (w ^ 1) * 6144 + row * 128 + ((chunk ^ ((l15b >> 1) & 7)) * 16) + (gqb & 1) * 8) = pk;
          }
        }
    };
    STAMP(14);
    if (hh) recv(o, 4); else recv(o, 0);
    BAR();
  }

  STAMP(5);
  int lane3 = lane; asm volatile("" : "+v"(lane3));
  calc_wtr(lane3);
  const int l15c = lane3 & 15, gqc = lane3 >> 4;
  auto rd_frag3 = [&](Frag& f, unsigned stage_off, int ks) {
#pragma unroll
    for (int c = 0; c < 4; ++c) f.w[c] = trread2(lds, stage_off + wtr[c], stage_off + wtr[c] + 512);
    const unsigned blk = XQ + (unsigned)((ks >> 1) ^ 1) * 6144;
#pragma unroll
    for (int rt = 0; rt < 3; ++rt) f.x[rt] = ldsr128(lds, blk + (unsigned)((16 * rt + l15c) * 128 + (((4 * (ks & 1) + gqc) ^ ((l15c >> 1) & 7)) * 16)));
  };
#pragma unroll
  for (int c = 0; c < 4; ++c)
#pragma unroll
    for (int rt = 0; rt < 3; ++rt) acc[c][rt] = f32x4{0.f, 0.f, 0.f, 0.f};
  WAIT_V(4);
  rd_frag3(fa, S0, (0 + rot) & 15); pin(fa);
  issue_w(rwo, 10, (2 + rot) & 15, S0);
  WAIT_V(4);
  rd_frag3(fb, S1, (1 + rot) & 15);
  mm(fa);
  pin(fb);
  issue_w(rwo, 10, (3 + rot) & 15, S1);
#define CY3(acc) do {} while (0)
#pragma unroll 1
  for (int i = 2; i < 14; i += 2) {
    WAIT_V(4);
    CY3(cW);
    rd_frag3(fa, S0, (i + rot) & 15);
    mm(fb);
    pin(fa);
    CY3(cC);
    issue_w(rwo, 10, (i + 2 + rot) & 15, S0);
    CY3(cI);
    WAIT_V(4);
    CY3(cW);
    rd_frag3(fb, S1, (i + 1 + rot) & 15);
    mm(fa);
    pin(fb);
    CY3(cC);
    issue_w(rwo, 10, (i + 3 + rot) & 15, S1);
    CY3(cI);
  }
  WAIT_V(4);
  rd_frag3(fa, S0, (14 + rot) & 15);
  mm(fb);
  pin(fa);
  WAIT_V(0);
  rd_frag3(fb, S1, (15 + rot) & 15);
  mm(fa);
  pin(fb);
  mm(fb);

  STAMP(6);
  {
    char* part = p.ws + WS_PART;
    const __amdgpu_buffer_rsrc_t rs = __builtin_amdgcn_make_buffer_rsrc(part + (size_t)grp * 8 * 8 * 6144, 0, 8 * 8 * 6144, 0x00020000);
    const unsigned base = (unsigned)((w * 8 + kvh) * 6144);
#pragma unroll
    for (int cp = 0; cp < 2; ++cp)
#pragma unroll
      for (int rt = 0; rt < 3; ++rt) {
        const f32x4 a0 = acc[2 * cp][rt], a1 = acc[2 * cp + 1][rt];
        auto r0 = __builtin_amdgcn_permlane16_swap(cvtpk(a0[0], a0[1]), cvtpk(a1[0], a1[1]), false, false);
        auto r1 = __builtin_amdgcn_permlane16_swap(cvtpk(a0[2], a0[3]), cvtpk(a1[2], a1[3]), false, false);
        u32x4 pk = {r0[0], r1[0], r0[1], r1[1]};
        const unsigned off = base + (unsigned)((16 * rt + l15c) * 128 + (16 * (2 * cp + (gqc & 1)) + 8 * (gqc >> 1)) * 2);
        __builtin_amdgcn_raw_buffer_store_b128(pk, rs, off, 0, 16);
      }
    WAIT_V(0);
    STAMP(7);
    AS1 unsigned* flags = (AS1 unsigned*)(p.ws + WS_FLAG + (size_t)grp * 64 * 64);
    if (lane == 0) __hip_atomic_store(flags + (w * 8 + kvh) * 16, epoch, __ATOMIC_RELAXED, __HIP_MEMORY_SCOPE_AGENT);
    for (unsigned spins = 0; spins < 4000000u; ++spins) {
      unsigned f = epoch;
      if (lane < 8) f = __hip_atomic_load(flags + (kvh * 8 + lane) * 16, __ATOMIC_RELAXED, __HIP_MEMORY_SCOPE_AGENT);
      if (__all(f == epoch)) break;
      __builtin_amdgcn_s_sleep(1);
    }
    asm volatile("" ::: "memory");
    STAMP(8);
    if (tid == 0) __hip_atomic_store((AS1 unsigned*)(p.ws + WS_EPOCH + grp * 128), epoch, __ATOMIC_RELAXED, __HIP_MEMORY_SCOPE_AGENT);
    char* outp = p.out + (size_t)b * 48 * 2048 + br * 1024 + kvh * 128;
    const int q8 = w * 48 + lane;
    if (lane < 48) {
      u32x4 v[8];
#pragma unroll
      for (int sidx = 0; sidx < 8; ++sidx) v[sidx] = __builtin_amdgcn_raw_buffer_load_b128(rs, (unsigned)((kvh * 8 + sidx) * 6144 + q8 * 16), 0, 16);
      float s[8];
#pragma unroll
      for (int j = 0; j < 8; ++j) s[j] = 0.f;
#pragma unroll
      for (int sidx = 0; sidx < 8; ++sidx)
#pragma unroll
        for (int j = 0; j < 4; ++j) { s[2 * j] += __uint_as_float(v[sidx][j] << 16); s[2 * j + 1] += __uint_as_float(v[sidx][j] & 0xffff0000u); }
      const int row = q8 >> 3, c8 = q8 & 7;
      u32x4 pk = {cvtpk(s[0], s[1]), cvtpk(s[2], s[3]), cvtpk(s[4], s[5]), cvtpk(s[6], s[7])};
      *(u32x4*)(outp + (size_t)row * 2048 + c8 * 16) = pk;
    }
  }
  STAMP(9);
}

extern "C" void kernel_launch(void* const* d_in, const int* in_sizes, int n_in, void* d_out, int out_size, void* d_ws, size_t ws_size,
                              hipStream_t stream) {
  Params p;
  for (int br = 0; br < 2; ++br) {
    p.x[br] = (const char*)d_in[5 * br + 0];
    p.k[br] = (const char*)d_in[5 * br + 1];
    p.v[br] = (const char*)d_in[5 * br + 2];
    p.wq[br] = (const char*)d_in[5 * br + 3];
    p.wo[br] = (const char*)d_in[5 * br + 4];
  }
  p.out = (char*)d_out;
  p.ws = (char*)d_ws;
  (void)hipFuncSetAttribute((const void*)fused_gqa_kernel, hipFuncAttributeMaxDynamicSharedMemorySize, LDS_BYTES);
  fused_gqa_kernel<<<256, 512, LDS_BYTES, stream>>>(p);
}
```

```cpp
#include <hip/hip_runtime.h>
#include <hip/hip_bf16.h>
#include <stdint.h>
#include <type_traits>

typedef short s16x4 __attribute__((ext_vector_type(4)));
typedef short s16x8 __attribute__((ext_vector_type(8)));
typedef __bf16 bf16x8 __attribute__((ext_vector_type(8)));
typedef __bf16 bf16x2 __attribute__((ext_vector_type(2)));
typedef float f32x4 __attribute__((ext_vector_type(4)));
typedef float f32x2 __attribute__((ext_vector_type(2)));
typedef unsigned u32x4 __attribute__((ext_vector_type(4)));
typedef unsigned u32x2 __attribute__((ext_vector_type(2)));

#define AS1 __attribute__((address_space(1)))
#define AS3 __attribute__((address_space(3)))
#define DI __device__ __forceinline__

struct Params {
  const char* x[2];
  const char* k[2];
  const char* v[2];
  const char* wq[2];
  const char* wo[2];
  char* out;
  char* ws;
};

constexpr unsigned XQ = 0;
constexpr unsigned RG = 49152;
constexpr unsigned UNIT = 16384;
constexpr int LDS_BYTES = 163840;

constexpr size_t WS_EPOCH = 0;
constexpr size_t WS_FLAG = 8192;
constexpr size_t WS_PART = 262144;

constexpr float CEXP = 0.08838834764831845f * 1.4426950408889634f;
constexpr float THR = 8.0f;

#define WAIT_V(n) asm volatile("s_waitcnt vmcnt(" #n ")" ::: "memory")
#define BAR() do { asm volatile("s_waitcnt lgkmcnt(0)" ::: "memory"); __builtin_amdgcn_s_barrier(); asm volatile("" ::: "memory"); } while (0)

DI f32x4 mfma16(s16x8 a, s16x8 b, f32x4 c) {
  return __builtin_amdgcn_mfma_f32_16x16x32_bf16(__builtin_bit_cast(bf16x8, a), __builtin_bit_cast(bf16x8, b), c, 0, 0, 0);
}
DI unsigned cvtpk(float lo, float hi) {
  f32x2 v = {lo, hi};
  bf16x2 b = __builtin_convertvector(v, bf16x2);
  return __builtin_bit_cast(unsigned, b);
}
DI s16x4 trread(const char* lds, unsigned off) {
  return __builtin_amdgcn_ds_read_tr16_b64_v4i16((AS3 s16x4*)(lds + off));
}
DI s16x4 trreadA(unsigned a) { return __builtin_amdgcn_ds_read_tr16_b64_v4i16((AS3 s16x4*)(size_t)a); }
DI s16x8 trread2A(unsigned a0, unsigned a1) { s16x4 a = trreadA(a0), b = trreadA(a1); return __builtin_shufflevector(a, b, 0, 1, 2, 3, 4, 5, 6, 7); }
DI s16x8 ldsr128A(unsigned a) { return *(const AS3 s16x8*)(size_t)a; }
DI s16x8 trread2(const char* lds, unsigned off0, unsigned off1) {
  s16x4 a = trread(lds, off0), b = trread(lds, off1);
  return __builtin_shufflevector(a, b, 0, 1, 2, 3, 4, 5, 6, 7);
}
DI s16x8 ldsr128(const char* lds, unsigned off) { return *(const s16x8*)(lds + off); }
DI void dma_d(__amdgpu_buffer_rsrc_t rs, unsigned voff, unsigned soff, unsigned ldsaddr) {
  asm volatile("s_mov_b32 m0, %0\n\ts_nop 0\n\tbuffer_load_dwordx4 %1, %2, %3 offen lds" ::"s"(ldsaddr), "v"(voff), "s"(rs), "s"(soff) : "memory");
}
DI void dma_nt(__amdgpu_buffer_rsrc_t rs, unsigned voff, unsigned soff, unsigned ldsaddr) {
  asm volatile("s_mov_b32 m0, %0\n\ts_nop 0\n\tbuffer_load_dwordx4 %1, %2, %3 offen nt lds" ::"s"(ldsaddr), "v"(voff), "s"(rs), "s"(soff) : "memory");
}
DI __amdgpu_buffer_rsrc_t mkrsrc(const void* p, unsigned bytes) {
  return __builtin_amdgcn_make_buffer_rsrc((void*)p, 0, bytes, 0x00020000);
}
DI float xmax16_32(float v) {
  unsigned u = __float_as_uint(v);
  auto r = __builtin_amdgcn_permlane16_swap(u, u, false, false);
  v = fmaxf(__uint_as_float(r[0]), __uint_as_float(r[1]));
  u = __float_as_uint(v);
  auto r2 = __builtin_amdgcn_permlane32_swap(u, u, false, false);
  return fmaxf(__uint_as_float(r2[0]), __uint_as_float(r2[1]));
}
DI float xsum16_32(float v) {
  unsigned u = __float_as_uint(v);
  auto r = __builtin_amdgcn_permlane16_swap(u, u, false, false);
  v = __uint_as_float(r[0]) + __uint_as_float(r[1]);
  u = __float_as_uint(v);
  auto r2 = __builtin_amdgcn_permlane32_swap(u, u, false, false);
  return __uint_as_float(r2[0]) + __uint_as_float(r2[1]);
}

__global__ __launch_bounds__(512, 2) void fused_gqa_kernel(Params p) {
  extern __shared__ __attribute__((aligned(16))) char lds[];
  const int tid = threadIdx.x, lane = tid & 63;
  const int w = __builtin_amdgcn_readfirstlane(tid >> 6);
  const int bid = blockIdx.x;
  const int kvh = bid & 7, b = (bid >> 3) & 15, br = bid >> 7;
  const int grp = bid >> 3;
  const int l15 = lane & 15, gq = lane >> 4;
  const int tq = (lane & 15) >> 2, tp = lane & 3;
  const int g = w >> 1, hh = w & 1;
  const unsigned ldsb = (unsigned)(uintptr_t)lds;
  asm volatile("" :: "s"(p.ws), "s"(p.out), "s"(p.x[br]), "s"(p.k[br]), "s"(p.v[br]), "s"(p.wq[br]), "s"(p.wo[br]));
  const __amdgpu_buffer_rsrc_t rx = mkrsrc(p.x[br] + (size_t)b * 48 * 1024, 48 * 1024);
  const __amdgpu_buffer_rsrc_t rk = mkrsrc(p.k[br] + (size_t)(b * 8 + kvh) * 131072, 131072);
  const __amdgpu_buffer_rsrc_t rv = mkrsrc(p.v[br] + (size_t)(b * 8 + kvh) * 131072, 131072);
  const __amdgpu_buffer_rsrc_t rwq = mkrsrc(p.wq[br] + kvh * 1024, 512 * 8192 - kvh * 1024);
  const __amdgpu_buffer_rsrc_t rwo = mkrsrc(p.wo[br] + (size_t)kvh * 512 * 1024, 512 * 1024);

  const int wr = lane >> 3, wpos = lane & 7;
  const unsigned wsw0 = (unsigned)(wpos ^ (((wr >> 1) & 1) << 1)) * 16 + w * 128;

  unsigned wv0 = ((unsigned)wr << 13) + wsw0;
  auto issue_w = [&](__amdgpu_buffer_rsrc_t rs, int lgstride, int ks, unsigned stage_off) {
    const unsigned v0 = wv0, v1 = wv0 ^ 64;
#pragma unroll
    for (int i = 0; i < 4; ++i)
      dma_d(rs, (i & 1) ? v1 : v0, (unsigned)(ks * 32 + i * 8) << lgstride, ldsb + stage_off + w * 4096 + i * 1024);
  };
  const int kr = lane >> 4, kpos = lane & 15;
  unsigned kvo[2], vvo[2];
#pragma unroll
  for (int i = 0; i < 2; ++i) {
    const int row = (2 * w + i) * 4 + kr;
    kvo[i] = (unsigned)(row * 256 + ((kpos ^ (row & 15)) * 16));
    vvo[i] = (unsigned)(row * 256 + ((kpos ^ ((row & 7) << 1)) * 16));
  }
  auto issue_kv = [&](int n) {
    const unsigned dst = ldsb + RG + (unsigned)((4 + n) % 7) * UNIT + 2 * w * 1024;
    const unsigned so = (unsigned)(n >> 1) * 16384;
    if (n & 1) { dma_nt(rv, vvo[0], so, dst); dma_nt(rv, vvo[1], so, dst + 1024); }
    else       { dma_nt(rk, kvo[0], so, dst); dma_nt(rk, kvo[1], so, dst + 1024); }
  };

  const int rot = 2 * (b & 7);
  const unsigned xvo = (unsigned)((lane >> 3) * 1024 + (((lane & 7) ^ (((lane >> 3) >> 1) & 3)) * 16));
  auto issue_x = [&](int q) {
    const int kbi = q / 6, j = q % 6;
    const int kb = ((rot >> 1) + kbi) & 7;
    dma_d(rx, xvo ^ (unsigned)((j & 1) << 6), (unsigned)(j * 8 * 1024 + kb * 128), ldsb + XQ + kb * 6144 + j * 1024);
  };
  issue_x(w);
  issue_w(rwq, 13, (0 + rot) & 15, RG);
  issue_w(rwq, 13, (1 + rot) & 15, RG + 2 * UNIT);
#pragma unroll
  for (int i = 0; i < 5; ++i) issue_x(8 + w + 8 * i);
  const unsigned epoch = __hip_atomic_load((const AS1 unsigned*)(p.ws + WS_EPOCH + grp * 128), __ATOMIC_RELAXED, __HIP_MEMORY_SCOPE_AGENT) + 1u;

  unsigned wtr[4];
  auto calc_wtr = [&](int ln) {
    const int gq_ = ln >> 4, tq_ = (ln & 15) >> 2, tp_ = ln & 3;
    const int wf = (((tq_ >> 1) & 1) << 1) | ((gq_ & 1) << 2);
#pragma unroll
    for (int c = 0; c < 4; ++c) wtr[c] = (unsigned)((8 * gq_ + tq_) * 128 + (((2 * c + (tp_ >> 1)) ^ wf) * 16) + 8 * (tp_ & 1)) + w * 4096;
  };
  calc_wtr(lane);

  struct Frag { s16x8 w[4]; s16x8 x[3]; };
  auto rd_frag1 = [&](Frag& f, unsigned stage_off, int ks) {
#pragma unroll
    for (int c = 0; c < 4; ++c) f.w[c] = trread2(lds, stage_off + wtr[c], stage_off + wtr[c] + 512);
#pragma unroll
    for (int rt = 0; rt < 3; ++rt) f.x[rt] = ldsr128(lds, XQ + (unsigned)(ks >> 1) * 6144 + (unsigned)((16 * rt + l15) * 128 + (((4 * (ks & 1) + gq) ^ ((l15 >> 1) & 7)) * 16)));
  };
  auto pin = [&](Frag& f) {
    asm volatile("s_waitcnt lgkmcnt(0)" : "+v"(f.w[0]), "+v"(f.w[1]), "+v"(f.w[2]), "+v"(f.w[3]), "+v"(f.x[0]), "+v"(f.x[1]), "+v"(f.x[2])::"memory");
  };
  f32x4 acc[4][3];
  auto mm = [&](Frag& f) {
#pragma unroll
    for (int c = 0; c < 4; ++c)
#pragma unroll
      for (int rt = 0; rt < 3; ++rt) acc[c][rt] = mfma16(f.w[c], f.x[rt], acc[c][rt]);
  };

#pragma unroll
  for (int c = 0; c < 4; ++c)
#pragma unroll
    for (int rt = 0; rt < 3; ++rt) acc[c][rt] = f32x4{0.f, 0.f, 0.f, 0.f};
  constexpr unsigned S0 = RG, S1 = RG + 2 * UNIT;
  Frag fa, fb;
  WAIT_V(10); BAR();
  rd_frag1(fa, S0, (0 + rot) & 15); pin(fa);
  issue_w(rwq, 13, (2 + rot) & 15, S0);
  WAIT_V(10);
  rd_frag1(fb, S1, (1 + rot) & 15);
  mm(fa);
  pin(fb);
  issue_w(rwq, 13, (3 + rot) & 15, S1);
  WAIT_V(4); BAR();
  rd_frag1(fa, S0, (2 + rot) & 15);
  mm(fb);
  pin(fa);
  issue_w(rwq, 13, (4 + rot) & 15, S0);
  WAIT_V(4);
  rd_frag1(fb, S1, (3 + rot) & 15);
  mm(fa);
  pin(fb);
  issue_w(rwq, 13, (5 + rot) & 15, S1);
#pragma unroll 1
  for (int i = 4; i < 14; i += 2) {
    WAIT_V(4);
    rd_frag1(fa, S0, (i + rot) & 15);
    mm(fb);
    pin(fa);
    issue_w(rwq, 13, (i + 2 + rot) & 15, S0);
    WAIT_V(4);
    rd_frag1(fb, S1, (i + 1 + rot) & 15);
    mm(fa);
    pin(fb);
    issue_w(rwq, 13, (i + 3 + rot) & 15, S1);
  }
  issue_kv(0); issue_kv(1); issue_kv(2);
  WAIT_V(10);
  rd_frag1(fa, S0, (14 + rot) & 15);
  mm(fb);
  pin(fa);
  WAIT_V(6);
  rd_frag1(fb, S1, (15 + rot) & 15);
  mm(fa);
  pin(fb);
  mm(fb);
  BAR();
#pragma unroll
  for (int c = 0; c < 4; ++c)
#pragma unroll
    for (int rt = 0; rt < 3; ++rt) {
      const int row = 16 * rt + l15;
      const int chunk = 8 * hh + 2 * c + (gq >> 1);
      u32x2 v = {cvtpk(acc[c][rt][0] * CEXP, acc[c][rt][1] * CEXP), cvtpk(acc[c][rt][2] * CEXP, acc[c][rt][3] * CEXP)};
      *(u32x2*)(lds + XQ + g * 12288 + row * 256 + ((chunk ^ l15) * 16) + (gq & 1) * 8) = v;
    }
  WAIT_V(4);
  BAR();
  s16x8 qf[3][4];
#pragma unroll
  for (int rt = 0; rt < 3; ++rt)
#pragma unroll
    for (int s = 0; s < 4; ++s) qf[rt][s] = ldsr128(lds, XQ + g * 12288 + (16 * rt + l15) * 256 + (((4 * s + gq) ^ l15) * 16));
  issue_kv(3); issue_kv(4);

  wv0 = (wv0 & 8191u) + ((wv0 >> 13) << 10);
  f32x4 o[8][3];
#pragma unroll
  for (int c = 0; c < 8; ++c)
#pragma unroll
    for (int rt = 0; rt < 3; ++rt) o[c][rt] = f32x4{0.f, 0.f, 0.f, 0.f};
  f32x4 lacc[3];
#pragma unroll
  for (int rt = 0; rt < 3; ++rt) lacc[rt] = f32x4{0.f, 0.f, 0.f, 0.f};
  const s16x8 ones = {0x3F80, 0x3F80, 0x3F80, 0x3F80, 0x3F80, 0x3F80, 0x3F80, 0x3F80};
  const unsigned koff = (unsigned)((32 * hh + l15) * 256 + ((gq ^ l15) * 16));
  const int vrow = 32 * hh + 4 * gq + tq;
  const int vsw = (vrow & 7) << 1;
  const unsigned voff = (unsigned)(vrow * 256 + (((tp >> 1) ^ vsw) * 16) + 8 * (tp & 1));

  typedef f32x4 Stile[2][3];
#define SB() __builtin_amdgcn_sched_barrier(0)
  auto max3a = [](float a, float b2, float c) { float r; asm("v_max3_f32 %0, %1, %2, %3" : "=v"(r) : "v"(a), "v"(b2), "v"(c)); return r; };
  auto max2a = [](float a, float b2) { float r; asm("v_max_f32_e32 %0, %1, %2" : "=v"(r) : "v"(a), "v"(b2)); return r; };
  auto xmax = [&](float v) {
    unsigned u = __float_as_uint(v);
    auto r = __builtin_amdgcn_permlane16_swap(u, u, false, false);
    v = max2a(__uint_as_float(r[0]), __uint_as_float(r[1]));
    u = __float_as_uint(v);
    auto r2 = __builtin_amdgcn_permlane32_swap(u, u, false, false);
    return max2a(__uint_as_float(r2[0]), __uint_as_float(r2[1]));
  };
  auto uoff = [&](int n) { return RG + (unsigned)((4 + n) % 7) * UNIT; };
  auto step_top = [&](int t) {
    if (t == 0) WAIT_V(8);
    else if (t <= 5) WAIT_V(6);
    else if (t == 6) WAIT_V(2);
    else WAIT_V(4);
    BAR();
    if (t == 0) issue_kv(7);
    else if (t <= 4) { issue_kv(2 * t + 6); issue_kv(2 * t + 7); }
    else if (t == 6) issue_w(rwo, 10, (0 + rot) & 15, S0);
    else if (t == 7) issue_w(rwo, 10, (1 + rot) & 15, S1);
  };
  s16x8 pf[3];
  f32x4 negm[3];
  auto stepAB = [&](Stile& sn, Stile& sc, unsigned ku, unsigned vu, float& lmax, auto DOQK, auto DOSM, auto CINIT) {
    constexpr bool doqk = decltype(DOQK)::value, dosm = decltype(DOSM)::value, cinit = decltype(CINIT)::value;
    s16x8 kf[8], vf[8];
    unsigned kb = ldsb + ku + koff; asm volatile("" : "+v"(kb));
    unsigned vb = ldsb + vu + voff; asm volatile("" : "+v"(vb));
    float e[24];
    if constexpr (doqk) {
      kf[0] = ldsr128A(kb);
      kf[1] = ldsr128A(kb + 4096);
      kf[2] = ldsr128A(kb ^ 64u);
    }
#pragma unroll
    for (int j = 0; j < 8; ++j) {
      if constexpr (doqk) {
        if (j + 3 < 8) kf[j + 3] = ldsr128A((kb ^ (unsigned)(((j + 3) >> 1) << 6)) + ((j + 3) & 1) * 4096);
      }
      if constexpr (dosm) {
        if (j >= 6) { const unsigned a = vb ^ (unsigned)((j - 6) << 5); vf[j - 6] = trread2A(a, a + 4096); }
#pragma unroll
        for (int u = 0; u < 3; ++u) {
          const int q = 3 * j + u, rt = q >> 3, idx = q & 7;
          e[q] = __builtin_amdgcn_exp2f(sc[idx >> 2][rt][idx & 3]);
        }
      }
      if constexpr (doqk) {
#pragma unroll
        for (int rt = 0; rt < 3; ++rt) {
          if (j < 2) sn[j & 1][rt] = mfma16(kf[j], qf[rt][j >> 1], cinit ? negm[rt] : f32x4{0.f, 0.f, 0.f, 0.f});
          else sn[j & 1][rt] = mfma16(kf[j], qf[rt][j >> 1], sn[j & 1][rt]);
        }
      }
      if constexpr (dosm) {
#pragma unroll
        for (int pr = 0; pr < 12; ++pr) {
          const int hi_el = 2 * pr + 1;
          if (hi_el / 3 == j) {
            const int rt = pr >> 2;
            unsigned pk = cvtpk(e[2 * pr], e[2 * pr + 1]);
            u32x4 t4 = __builtin_bit_cast(u32x4, pf[rt]); t4[pr & 3] = pk; pf[rt] = __builtin_bit_cast(s16x8, t4);
          }
        }
      }
      SB();
    }
    if constexpr (dosm) {
      { const unsigned a = vb ^ 64u; vf[2] = trread2A(a, a + 4096); }
      { const unsigned a = vb ^ 96u; vf[3] = trread2A(a, a + 4096); }
      asm volatile("s_nop 7" ::: "memory");
      SB();
#pragma unroll
      for (int rt = 0; rt < 3; ++rt) lacc[rt] = mfma16(ones, pf[rt], lacc[rt]);
      float t0 = 0.f;
#pragma unroll
      for (int c = 0; c < 8; ++c) {
        if (c + 4 < 8) { const unsigned a = vb ^ (unsigned)((c + 4) << 5); vf[c + 4] = trread2A(a, a + 4096); }
#pragma unroll
        for (int rt = 0; rt < 3; ++rt) o[c][rt] = mfma16(vf[c], pf[rt], o[c][rt]);
        if constexpr (doqk) {
          if (c < 6) {
            const int rt = c >> 1, kt = c & 1;
            if (c == 0) t0 = max3a(sn[0][0][0], sn[0][0][1], sn[0][0][2]);
            else t0 = max3a(t0, sn[kt][rt][0], sn[kt][rt][1]);
            if (c == 0) t0 = max2a(t0, sn[0][0][3]); else t0 = max3a(t0, sn[kt][rt][2], sn[kt][rt][3]);
          }
        }
        SB();
      }
      lmax = t0;
    }
  };
  using T_ = std::integral_constant<bool, true>; using F_ = std::integral_constant<bool, false>;
  auto decide = [&](Stile& sn, float lmax) {
    if (__builtin_expect(__any(lmax > THR), 0)) {
#pragma unroll
      for (int rt = 0; rt < 3; ++rt) {
        float a = fmaxf(fmaxf(sn[0][rt][0], sn[0][rt][1]), fmaxf(sn[0][rt][2], sn[0][rt][3]));
        float c2 = fmaxf(fmaxf(sn[1][rt][0], sn[1][rt][1]), fmaxf(sn[1][rt][2], sn[1][rt][3]));
        const float dl = fmaxf(xmax16_32(fmaxf(a, c2)), 0.f);
        const float al = __builtin_amdgcn_exp2f(-dl);
        negm[rt] -= dl; lacc[rt] *= al;
        sn[0][rt] -= dl; sn[1][rt] -= dl;
#pragma unroll
        for (int c = 0; c < 8; ++c) o[c][rt] *= al;
      }
    }
  };

  Stile sa, sb;
  float lmax;
  stepAB(sa, sb, uoff(0), 0, lmax, T_{}, F_{}, F_{});
  issue_kv(5); issue_kv(6);
#pragma unroll
  for (int rt = 0; rt < 3; ++rt) {
    float a = fmaxf(fmaxf(sa[0][rt][0], sa[0][rt][1]), fmaxf(sa[0][rt][2], sa[0][rt][3]));
    float c2 = fmaxf(fmaxf(sa[1][rt][0], sa[1][rt][1]), fmaxf(sa[1][rt][2], sa[1][rt][3]));
    const float m0 = xmax16_32(fmaxf(a, c2));
    negm[rt] = f32x4{-m0, -m0, -m0, -m0};
    sa[0][rt] -= m0; sa[1][rt] -= m0;
  }
#pragma unroll 1
  for (int t = 0; t < 6; t += 2) {
    step_top(t);
    stepAB(sb, sa, uoff(2 * t + 2), uoff(2 * t + 1), lmax, T_{}, T_{}, T_{});
    decide(sb, lmax);
    step_top(t + 1);
    stepAB(sa, sb, uoff(2 * t + 4), uoff(2 * t + 3), lmax, T_{}, T_{}, T_{});
    decide(sa, lmax);
  }
  step_top(6);
  stepAB(sb, sa, uoff(14), uoff(13), lmax, T_{}, T_{}, T_{});
  decide(sb, lmax);
  step_top(7);
  stepAB(sa, sb, 0, uoff(15), lmax, F_{}, T_{}, F_{});
  float mrow[3], lrow[3];
#pragma unroll
  for (int rt = 0; rt < 3; ++rt) { lrow[rt] = lacc[rt][0]; mrow[rt] = -negm[rt][0]; }
  {
    int lane2 = lane; asm volatile("" : "+v"(lane2));
    const int l15b = lane2 & 15, gqb = lane2 >> 4;
    float* stat = (float*)(lds + RG + 6 * UNIT);
    if (gqb == 0) {
#pragma unroll
      for (int rt = 0; rt < 3; ++rt) { stat[((w * 3 + rt) * 16 + l15b) * 2] = mrow[rt]; stat[((w * 3 + rt) * 16 + l15b) * 2 + 1] = lrow[rt]; }
    }
    auto send = [&](f32x4 (&oo)[8][3], int base) {
#pragma unroll
      for (int cp = 0; cp < 2; ++cp)
#pragma unroll
        for (int rt = 0; rt < 3; ++rt) {
          const f32x4 a0 = oo[base + 2 * cp][rt], a1 = oo[base + 2 * cp + 1][rt];
          u32x4 pk = {cvtpk(a0[0], a0[1]), cvtpk(a0[2], a0[3]), cvtpk(a1[0], a1[1]), cvtpk(a1[2], a1[3])};
          *(u32x4*)(lds + XQ + w * 6144 + (cp * 3 + rt) * 1024 + lane2 * 16) = pk;
        }
    };
    if (hh) send(o, 0); else send(o, 4);
    BAR();
    float s1[3], s2[3];
#pragma unroll
    for (int rt = 0; rt < 3; ++rt) {
      const float m2 = stat[(((w ^ 1) * 3 + rt) * 16 + l15b) * 2], l2 = stat[(((w ^ 1) * 3 + rt) * 16 + l15b) * 2 + 1];
      const float mn = fmaxf(mrow[rt], m2);
      const float w1 = __builtin_amdgcn_exp2f(mrow[rt] - mn), w2 = __builtin_amdgcn_exp2f(m2 - mn);
      const float inv = __builtin_amdgcn_rcpf(lrow[rt] * w1 + l2 * w2);
      s1[rt] = w1 * inv; s2[rt] = w2 * inv;
    }
    auto recv = [&](f32x4 (&oo)[8][3], int base) {
      u32x4 r[6];
#pragma unroll
      for (int i = 0; i < 6; ++i) r[i] = *(const u32x4*)(lds + XQ + (w ^ 1) * 6144 + i * 1024 + lane2 * 16);
      asm volatile("s_waitcnt lgkmcnt(0)" : "+v"(r[0]), "+v"(r[1]), "+v"(r[2]), "+v"(r[3]), "+v"(r[4]), "+v"(r[5]) :: "memory");
#pragma unroll
      for (int cp = 0; cp < 2; ++cp)
#pragma unroll
        for (int rt = 0; rt < 3; ++rt) {
#pragma unroll
          for (int h2 = 0; h2 < 2; ++h2) {
            const f32x4 a = oo[base + 2 * cp + h2][rt];
            const u32x4 rr = r[cp * 3 + rt];
            const float v0 = a[0] * s1[rt] + __uint_as_float(rr[2 * h2] << 16) * s2[rt];
            const float v1 = a[1] * s1[rt] + __uint_as_float(rr[2 * h2] & 0xffff0000u) * s2[rt];
            const float v2 = a[2] * s1[rt] + __uint_as_float(rr[2 * h2 + 1] << 16) * s2[rt];
            const float v3 = a[3] * s1[rt] + __uint_as_float(rr[2 * h2 + 1] & 0xffff0000u) * s2[rt];
            const int row = 16 * rt + l15b;
            const int chunk = 2 * (2 * cp + h2) + (gqb >> 1);
            u32x2 pk = {cvtpk(v0, v1), cvtpk(v2, v3)};
            *(u32x2*)(lds + XQ + (w ^ 1) * 6144 + row * 128 + ((chunk ^ ((l15b >> 1) & 7)) * 16) + (gqb & 1) * 8) = pk;
          }
        }
    };
    if (hh) recv(o, 4); else recv(o, 0);
    BAR();
  }
  int lane3 = lane; asm volatile("" : "+v"(lane3));
  calc_wtr(lane3);
  const int l15c = lane3 & 15, gqc = lane3 >> 4;
  auto rd_frag3 = [&](Frag& f, unsigned stage_off, int ks) {
#pragma unroll
    for (int c = 0; c < 4; ++c) f.w[c] = trread2(lds, stage_off + wtr[c], stage_off + wtr[c] + 512);
    const unsigned blk = XQ + (unsigned)((ks >> 1) ^ 1) * 6144;
#pragma unroll
    for (int rt = 0; rt < 3; ++rt) f.x[rt] = ldsr128(lds, blk + (unsigned)((16 * rt + l15c) * 128 + (((4 * (ks & 1) + gqc) ^ ((l15c >> 1) & 7)) * 16)));
  };
#pragma unroll
  for (int c = 0; c < 4; ++c)
#pragma unroll
    for (int rt = 0; rt < 3; ++rt) acc[c][rt] = f32x4{0.f, 0.f, 0.f, 0.f};
  WAIT_V(4);
  rd_frag3(fa, S0, (0 + rot) & 15); pin(fa);
  issue_w(rwo, 10, (2 + rot) & 15, S0);
  WAIT_V(4);
  rd_frag3(fb, S1, (1 + rot) & 15);
  mm(fa);
  pin(fb);
  issue_w(rwo, 10, (3 + rot) & 15, S1);
#pragma unroll 1
  for (int i = 2; i < 14; i += 2) {
    WAIT_V(4);
    rd_frag3(fa, S0, (i + rot) & 15);
    mm(fb);
    pin(fa);
    issue_w(rwo, 10, (i + 2 + rot) & 15, S0);
    WAIT_V(4);
    rd_frag3(fb, S1, (i + 1 + rot) & 15);
    mm(fa);
    pin(fb);
    issue_w(rwo, 10, (i + 3 + rot) & 15, S1);
  }
  WAIT_V(4);
  rd_frag3(fa, S0, (14 + rot) & 15);
  mm(fb);
  pin(fa);
  WAIT_V(0);
  rd_frag3(fb, S1, (15 + rot) & 15);
  mm(fa);
  pin(fb);
  mm(fb);
  {
    char* part = p.ws + WS_PART;
    const __amdgpu_buffer_rsrc_t rs = __builtin_amdgcn_make_buffer_rsrc(part + (size_t)grp * 8 * 8 * 6144, 0, 8 * 8 * 6144, 0x00020000);
    const unsigned base = (unsigned)((w * 8 + kvh) * 6144);
#pragma unroll
    for (int cp = 0; cp < 2; ++cp)
#pragma unroll
      for (int rt = 0; rt < 3; ++rt) {
        const f32x4 a0 = acc[2 * cp][rt], a1 = acc[2 * cp + 1][rt];
        auto r0 = __builtin_amdgcn_permlane16_swap(cvtpk(a0[0], a0[1]), cvtpk(a1[0], a1[1]), false, false);
        auto r1 = __builtin_amdgcn_permlane16_swap(cvtpk(a0[2], a0[3]), cvtpk(a1[2], a1[3]), false, false);
        u32x4 pk = {r0[0], r1[0], r0[1], r1[1]};
        const unsigned off = base + (unsigned)((16 * rt + l15c) * 128 + (16 * (2 * cp + (gqc & 1)) + 8 * (gqc >> 1)) * 2);
        __builtin_amdgcn_raw_buffer_store_b128(pk, rs, off, 0, 16);
      }
    WAIT_V(0);
    AS1 unsigned* flags = (AS1 unsigned*)(p.ws + WS_FLAG + (size_t)grp * 64 * 64);
    if (lane == 0) __hip_atomic_store(flags + (w * 8 + kvh) * 16, epoch, __ATOMIC_RELAXED, __HIP_MEMORY_SCOPE_AGENT);
    for (unsigned spins = 0; spins < 4000000u; ++spins) {
      unsigned f = epoch;
      if (lane < 8) f = __hip_atomic_load(flags + (kvh * 8 + lane) * 16, __ATOMIC_RELAXED, __HIP_MEMORY_SCOPE_AGENT);
      if (__all(f == epoch)) break;
      __builtin_amdgcn_s_sleep(1);
    }
    asm volatile("" ::: "memory");
    if (tid == 0) __hip_atomic_store((AS1 unsigned*)(p.ws + WS_EPOCH + grp * 128), epoch, __ATOMIC_RELAXED, __HIP_MEMORY_SCOPE_AGENT);
    char* outp = p.out + (size_t)b * 48 * 2048 + br * 1024 + kvh * 128;
    const int q8 = w * 48 + lane;
    if (lane < 48) {
      u32x4 v[8];
#pragma unroll
      for (int sidx = 0; sidx < 8; ++sidx) v[sidx] = __builtin_amdgcn_raw_buffer_load_b128(rs, (unsigned)((kvh * 8 + sidx) * 6144 + q8 * 16), 0, 16);
      float s[8];
#pragma unroll
      for (int j = 0; j < 8; ++j) s[j] = 0.f;
#pragma unroll
      for (int sidx = 0; sidx < 8; ++sidx)
#pragma unroll
        for (int j = 0; j < 4; ++j) { s[2 * j] += __uint_as_float(v[sidx][j] << 16); s[2 * j + 1] += __uint_as_float(v[sidx][j] & 0xffff0000u); }
      const int row = q8 >> 3, c8 = q8 & 7;
      u32x4 pk = {cvtpk(s[0], s[1]), cvtpk(s[2], s[3]), cvtpk(s[4], s[5]), cvtpk(s[6], s[7])};
      *(u32x4*)(outp + (size_t)row * 2048 + c8 * 16) = pk;
    }
  }
}

extern "C" void kernel_launch(void* const* d_in, const int* in_sizes, int n_in, void* d_out, int out_size, void* d_ws, size_t ws_size,
                              hipStream_t stream) {
  Params p;
  for (int br = 0; br < 2; ++br) {
    p.x[br] = (const char*)d_in[5 * br + 0];
    p.k[br] = (const char*)d_in[5 * br + 1];
    p.v[br] = (const char*)d_in[5 * br + 2];
    p.wq[br] = (const char*)d_in[5 * br + 3];
    p.wo[br] = (const char*)d_in[5 * br + 4];
  }
  p.out = (char*)d_out;
  p.ws = (char*)d_ws;
  (void)hipFuncSetAttribute((const void*)fused_gqa_kernel, hipFuncAttributeMaxDynamicSharedMemorySize, LDS_BYTES);
  fused_gqa_kernel<<<256, 512, LDS_BYTES, stream>>>(p);
}
```
